```python
import torch

def _softmax(x, axis=-1):
    return torch.softmax(x, dim=axis)
def _gelu(x):
    return torch.nn.functional.gelu(x, approximate="tanh")
def _silu(x):
    return torch.nn.functional.silu(x)
def _sigmoid(x):
    return torch.sigmoid(x)
def _layernorm(x, axis=-1, eps=1e-5):
    m = x.mean(dim=axis, keepdim=True)
    v = x.var(dim=axis, keepdim=True, unbiased=False)
    return (x - m) / torch.sqrt(v + eps)
def _scatter_add(tgt, idx, src, axis=0):
    return tgt.clone().index_add_(axis, idx, src)
def _segment_sum(src, seg, n):
    out = torch.zeros((n,) + src.shape[1:], dtype=src.dtype)
    return out.index_add_(0, seg, src)
def _pad(x, pw):
    flat = tuple(v for pair in reversed(pw) for v in pair)
    return torch.nn.functional.pad(x, flat)

kernel_name = 'standalone__hybrid_graph_tf_nagphormer__graph'

def setup_inputs():

    _w = (lambda a: (lambda t: t.to(torch.bfloat16) if t.is_floating_point() else t)(torch.as_tensor(a)))
    N, deg, D = 12500, 32, 128
    E = N * deg
    src = (lambda lo, hi, *s: torch.randint(lo, hi, s))(0, N, E).to(torch.int64)
    dst = (lambda lo, hi, *s: torch.randint(lo, hi, s))(0, N, E).to(torch.int64)
    deg_cnt = torch.bincount(dst, minlength=N).to(float) + 1.0
    sc = 1.0 / D ** 0.5
    return {
        "x": _w((lambda *s: torch.randn(*s, dtype=torch.bfloat16) * 0.1)(N, D)),
        "src": _w(src),
        "dst": _w(dst),
        "deg_inv_sqrt": _w(1.0 / (lambda _x: torch.sqrt(torch.as_tensor(_x)))(deg_cnt)),
        "wq": _w((lambda *s: torch.randn(*s, dtype=torch.bfloat16) * 1.6)(D, D) * sc),
        "wk": _w((lambda *s: torch.randn(*s, dtype=torch.bfloat16) * 1.6)(D, D) * sc),
        "wv": _w((lambda *s: torch.randn(*s, dtype=torch.bfloat16) * 0.1)(D, D) * sc),
        "wo": _w((lambda *s: torch.randn(*s, dtype=torch.bfloat16) * 0.1)(D, D) * sc),
        "w_r": _w((lambda *s: torch.randn(*s, dtype=torch.bfloat16) * 0.1)(D)),
    }

def reference(x, src, dst, deg_inv_sqrt, wq, wk, wv, wo, w_r):
    N, D = x.shape[0], x.shape[1]
    K = 7
    h = x
    toks = [x]
    for _ in range(K):
        msg = h[src] * deg_inv_sqrt[src, None] * deg_inv_sqrt[dst, None]
        h = _segment_sum(msg, dst, N)
        toks.append(h)
    tok = torch.stack(toks, axis=1)
    t = _layernorm(tok, axis=-1)
    q   = torch.einsum('nkd,de->nke', t, wq)
    key = torch.einsum('nkd,de->nke', t, wk)
    v   = torch.einsum('nkd,de->nke', t, wv)
    scale = 1.0 / (D ** 0.5)
    attn = _softmax(torch.einsum('nkd,nld->nkl', q, key) * scale, axis=-1)
    o = torch.einsum('nkl,nld->nkd', attn, v)
    o = tok + torch.einsum('nkd,de->nke', o, wo)
    r = _layernorm(o, axis=-1)
    alpha = _softmax(torch.einsum('nkd,d->nk', r, w_r), axis=-1)
    return torch.einsum('nk,nkd->nd', alpha, o)
```

```cpp
#include <hip/hip_runtime.h>
#include <hip/hip_bf16.h>
#include <stdint.h>

typedef unsigned short u16;
__device__ __forceinline__ float b2f(u16 v) { return __uint_as_float((unsigned)v << 16); }
__device__ __forceinline__ u16 f2b(float f) { __hip_bfloat16 h = __float2bfloat16(f); return *(u16*)&h; }

constexpr int N = 12500, E = 400000, D = 128, KH = 7;

__global__ void k_count(const int* dst, int* cnt) { int e = blockIdx.x * 256 + threadIdx.x; if (e < E) atomicAdd(&cnt[dst[e]], 1); }
__global__ void k_scan(const int* cnt, int* off, int* cur) {
  __shared__ int s[1024];
  int t = threadIdx.x; int per = (N + 1023) / 1024; int a = t * per; int sum = 0;
  for (int i = a; i < a + per && i < N; ++i) sum += cnt[i];
  s[t] = sum; __syncthreads();
  for (int o = 1; o < 1024; o <<= 1) { int v = t >= o ? s[t - o] : 0; __syncthreads(); s[t] += v; __syncthreads(); }
  int base = s[t] - sum;
  for (int i = a; i < a + per && i < N; ++i) { off[i] = base; cur[i] = base; base += cnt[i]; }
  if (t == 1023) off[N] = s[1023];
}
__global__ void k_fill(const int* src, const int* dst, int* cur, int* col) { int e = blockIdx.x * 256 + threadIdx.x; if (e < E) { int p = atomicAdd(&cur[dst[e]], 1); col[p] = src[e]; } }
__global__ void k_hop(const u16* h, u16* hn, const int* off, const int* col, const u16* dis) {
  int n = blockIdx.x * 4 + (threadIdx.x >> 6); int l = threadIdx.x & 63; if (n >= N) return;
  float a0 = 0, a1 = 0;
  for (int i = off[n]; i < off[n + 1]; ++i) { int s = col[i]; float d = b2f(dis[s]); unsigned v = *(const unsigned*)(h + s * D + 2 * l); a0 += b2f(v & 0xffff) * d; a1 += b2f(v >> 16) * d; }
  float dd = b2f(dis[n]);
  *(unsigned*)(hn + n * D + 2 * l) = (unsigned)f2b(a0 * dd) | ((unsigned)f2b(a1 * dd) << 16);
}
__global__ void k_tf(const u16* x, const u16* hs, const u16* wq, const u16* wk, const u16* wv, const u16* wo, const u16* wr, u16* out) {
  __shared__ float tok[8][128], t[8][128], q[8][128], k[8][128], v[8][128], o[8][128], at[8][8], al[8];
  int n = blockIdx.x, d = threadIdx.x;
  for (int j = 0; j < 8; ++j) tok[j][d] = b2f(j == 0 ? x[n * D + d] : hs[(size_t)(j - 1) * N * D + n * D + d]);
  __syncthreads();
  for (int j = 0; j < 8; ++j) { float m = 0, vv = 0; for (int i = 0; i < D; ++i) m += tok[j][i]; m /= D; for (int i = 0; i < D; ++i) vv += (tok[j][i] - m) * (tok[j][i] - m); vv /= D; t[j][d] = (tok[j][d] - m) * rsqrtf(vv + 1e-5f); }
  __syncthreads();
  for (int j = 0; j < 8; ++j) { float a = 0, b = 0, c = 0; for (int i = 0; i < D; ++i) { a += t[j][i] * b2f(wq[i * D + d]); b += t[j][i] * b2f(wk[i * D + d]); c += t[j][i] * b2f(wv[i * D + d]); } q[j][d] = a; k[j][d] = b; v[j][d] = c; }
  __syncthreads();
  if (d < 64) { int a = d >> 3, b = d & 7; float s = 0; for (int i = 0; i < D; ++i) s += q[a][i] * k[b][i]; at[a][b] = s * 0.08838834764f; }
  __syncthreads();
  if (d < 8) { float m = -1e30f; for (int b = 0; b < 8; ++b) m = fmaxf(m, at[d][b]); float s = 0; for (int b = 0; b < 8; ++b) { at[d][b] = expf(at[d][b] - m); s += at[d][b]; } for (int b = 0; b < 8; ++b) at[d][b] /= s; }
  __syncthreads();
  for (int j = 0; j < 8; ++j) { float a = 0; for (int b = 0; b < 8; ++b) a += at[j][b] * v[b][d]; q[j][d] = a; }
  __syncthreads();
  for (int j = 0; j < 8; ++j) { float a = 0; for (int i = 0; i < D; ++i) a += q[j][i] * b2f(wo[i * D + d]); o[j][d] = tok[j][d] + a; }
  __syncthreads();
  if (d < 8) { int j = d; float m = 0, vv = 0; for (int i = 0; i < D; ++i) m += o[j][i]; m /= D; for (int i = 0; i < D; ++i) vv += (o[j][i] - m) * (o[j][i] - m); vv /= D; float r = rsqrtf(vv + 1e-5f); float a = 0; for (int i = 0; i < D; ++i) a += (o[j][i] - m) * r * b2f(wr[i]); al[j] = a; }
  __syncthreads();
  float m = -1e30f; for (int j = 0; j < 8; ++j) m = fmaxf(m, al[j]); float s = 0, acc = 0; for (int j = 0; j < 8; ++j) { float e = expf(al[j] - m); s += e; acc += e * o[j][d]; }
  out[n * D + d] = f2b(acc / s);
}

extern "C" void kernel_launch(void* const* d_in, const int* in_sizes, int n_in, void* d_out, int out_size, void* d_ws, size_t ws_size, hipStream_t stream) {
  const u16* x = (const u16*)d_in[0]; const int* src = (const int*)d_in[1]; const int* dst = (const int*)d_in[2]; const u16* dis = (const u16*)d_in[3];
  char* ws = (char*)d_ws; int* cnt = (int*)ws; int* off = cnt + 16384; int* cur = off + 16384; int* col = cur + 16384; u16* hs = (u16*)(ws + (4 << 20));
  hipMemsetAsync(cnt, 0, 16384 * 4, stream);
  k_count<<<(E + 255) / 256, 256, 0, stream>>>(dst, cnt);
  k_scan<<<1, 1024, 0, stream>>>(cnt, off, cur);
  k_fill<<<(E + 255) / 256, 256, 0, stream>>>(src, dst, cur, col);
  for (int h = 0; h < KH; ++h) k_hop<<<(N + 3) / 4, 256, 0, stream>>>(h == 0 ? x : hs + (size_t)(h - 1) * N * D, hs + (size_t)h * N * D, off, col, dis);
  k_tf<<<N, 128, 0, stream>>>(x, hs, (const u16*)d_in[4], (const u16*)d_in[5], (const u16*)d_in[6], (const u16*)d_in[7], (const u16*)d_in[8], (u16*)d_out);
}
```

```cpp
#include <hip/hip_runtime.h>
#include <stdint.h>

typedef unsigned short u16;
typedef unsigned char u8;
typedef unsigned long long u64;
typedef __bf16 v8bf __attribute__((ext_vector_type(8)));
typedef __bf16 v4bf __attribute__((ext_vector_type(4)));
typedef __bf16 v2bf __attribute__((ext_vector_type(2)));
typedef float v4f __attribute__((ext_vector_type(4)));
typedef float v2f __attribute__((ext_vector_type(2)));
typedef unsigned v4u __attribute__((ext_vector_type(4)));
typedef unsigned v2u __attribute__((ext_vector_type(2)));
typedef int v4i __attribute__((ext_vector_type(4)));
typedef u16 v2h __attribute__((ext_vector_type(2)));
#define LAS __attribute__((address_space(3)))
#define GAS __attribute__((address_space(1)))
#define DEVI __device__ __forceinline__

constexpr int NN = 12500, NE = 400000;
constexpr int NCH = 200;
constexpr int NPOS = NCH * 64;
constexpr int SLB = NPOS * 8;
constexpr int HOPB = 32 * SLB;
constexpr int G1 = 196, CAP = 40;
constexpr int SEGB = G1 * CAP * 4;
constexpr int NODEWG = 98;
constexpr size_t WS_B = 0;
constexpr size_t WS_SEG = WS_B + (size_t)9 * HOPB;
constexpr size_t WS_LIST = WS_SEG + (size_t)NCH * SEGB;
constexpr size_t WS_WF = WS_LIST + (size_t)NCH * 8192;
constexpr size_t WS_POS = WS_WF + 65536;
constexpr size_t WS_INV = WS_POS + 25600;
constexpr size_t WS_PDIS = WS_INV + 25600;
constexpr size_t WS_META = WS_PDIS + 51200;
constexpr size_t WS_O1C = WS_META + 4096;
constexpr size_t WS_O1 = WS_O1C + 1024;
constexpr size_t WS_O2 = WS_O1 + (size_t)G1 * 2048 * 4;
constexpr size_t WS_DBG = WS_O2 + (size_t)NCH * SEGB;

#define SPINCAP (1u << 22)
#define STAMPG(kern, i) do {} while (0)

DEVI LAS char* ldsbase(int a) { asm volatile("" : "+v"(a)); return (LAS char*)a; }
DEVI float bflo(unsigned u) { return __uint_as_float(u << 16); }
DEVI float bfhi(unsigned u) { return __uint_as_float(u & 0xffff0000u); }
DEVI unsigned pkbf(float a, float b) { v2f v = {a, b}; return __builtin_bit_cast(unsigned, __builtin_convertvector(v, v2bf)); }
DEVI __amdgpu_buffer_rsrc_t mkrsrc(const void* p, unsigned bytes) { return __builtin_amdgcn_make_buffer_rsrc((void*)p, 0, bytes, 0x00020000); }
template <int C> DEVI unsigned dppu(unsigned x) { return (unsigned)__builtin_amdgcn_update_dpp(0, (int)x, C, 0xF, 0xF, true); }
template <int C> DEVI float dppf(float x) { return __uint_as_float(dppu<C>(__float_as_uint(x))); }
constexpr int DPP_X1 = 0xB1, DPP_X2 = 0x4E, DPP_HM = 0x141, DPP_RM = 0x140;
DEVI float add16(float x) { v2u r = __builtin_amdgcn_permlane16_swap(__float_as_uint(x), __float_as_uint(x), false, false); return __uint_as_float(r[0]) + __uint_as_float(r[1]); }
DEVI float add32(float x) { v2u r = __builtin_amdgcn_permlane32_swap(__float_as_uint(x), __float_as_uint(x), false, false); return __uint_as_float(r[0]) + __uint_as_float(r[1]); }
DEVI float max16(float x) { v2u r = __builtin_amdgcn_permlane16_swap(__float_as_uint(x), __float_as_uint(x), false, false); return fmaxf(__uint_as_float(r[0]), __uint_as_float(r[1])); }
DEVI unsigned or16(unsigned x) { v2u r = __builtin_amdgcn_permlane16_swap(x, x, false, false); return r[0] | r[1]; }

DEVI unsigned key2(unsigned d, unsigned idx2) {
  v2h v = __builtin_bit_cast(v2h, d);
  v2h t = __builtin_elementwise_sub_sat((v2h){0x3F80, 0x3F80}, v);
  t = __builtin_elementwise_min(t, (v2h){4095, 4095});
  return (__builtin_bit_cast(unsigned, t) << 3) | idx2;
}
DEVI v4u keys8(v4u d) { return (v4u){key2(d[0], 0x00010000u), key2(d[1], 0x00030002u), key2(d[2], 0x00050004u), key2(d[3], 0x00070006u)}; }
DEVI unsigned rank8(v4u k, unsigned own) {
  const v2h o = __builtin_bit_cast(v2h, own | (own << 16));
  unsigned t[4];
#pragma unroll
  for (int i = 0; i < 4; ++i) { const unsigned ki = k[i]; const v2h df = __builtin_bit_cast(v2h, ki) - o; t[i] = __builtin_bit_cast(unsigned, df); }
  const unsigned a = __builtin_amdgcn_perm(t[0], t[1], 0x07050301u) & 0x80808080u;
  const unsigned b = __builtin_amdgcn_perm(t[2], t[3], 0x07050301u) & 0x80808080u;
  return __builtin_popcount(a) + __builtin_popcount(b);
}
DEVI unsigned chunk_of(unsigned w, unsigned r) {
  const u64 t0 = 0x150D0E09100C0800ull, t1 = 0x1611120A14020504ull, t2 = 0x17130F0B06070301ull;
  const unsigned j = w >> 3;
  const u64 t = j == 0 ? t0 : j == 1 ? t1 : t2;
  const unsigned slot = j == 3 ? 24u : (unsigned)(t >> (8 * r)) & 31u;
  return ((w + r) & 7) * 25 + slot;
}

constexpr int K1_LDS = 66 * 1024;
__global__ void __launch_bounds__(512, 2) k_prep(const u16* __restrict__ x, const int* __restrict__ src, const int* __restrict__ dst, const u16* __restrict__ dis,
                                                 const u16* __restrict__ wq, const u16* __restrict__ wk, const u16* __restrict__ wv, const u16* __restrict__ wo, char* __restrict__ ws) {
  extern __shared__ __attribute__((aligned(16))) char lds[];
  const int tid = threadIdx.x, b = blockIdx.x;
  STAMPG(0, 0);
  const __amdgpu_buffer_rsrc_t rdis = mkrsrc(dis, NN * 2);
  if (b < G1) {
    u16* keyL = (u16*)lds;
    unsigned* segL = (unsigned*)(lds + 25600);
    unsigned* cntL = (unsigned*)(lds + 25600 + 32000);
    u8* lutL = (u8*)(lds + 25600 + 32000 + 1024);
    const int q = b * 512 + tid;
    const bool act = q < NE / 4;
    v4i s4 = {0, 0, 0, 0}, d4 = {0, 0, 0, 0};
    if (act) { d4 = ((const v4i*)dst)[q]; s4 = ((const v4i*)src)[q]; }
    v4u kd[4];
#pragma unroll
    for (int i = 0; i < 4; ++i) kd[i] = __builtin_amdgcn_raw_buffer_load_b128(rdis, (tid + 512 * i) * 16, 0, 0);
#pragma unroll
    for (int i = 0; i < 4; ++i) { const int o = tid + 512 * i; if (o < 2000) ((v4u*)segL)[o] = (v4u){~0u, ~0u, ~0u, ~0u}; }
    if (tid < 256) cntL[tid] = 0;
    if (tid < 200) lutL[tid] = (u8)chunk_of(tid >> 3, tid & 7);
#pragma unroll
    for (int i = 0; i < 4; ++i) { const int o = tid + 512 * i; if (o < 1568) ((v4u*)keyL)[o] = keys8(kd[i]); }
    __syncthreads();
    STAMPG(0, 1);
    if (act) {
      unsigned cid[4], sl[4];
#pragma unroll
      for (int i = 0; i < 4; ++i) { const unsigned n = d4[i]; const unsigned r = rank8(((const v4u*)keyL)[n >> 3], keyL[n]); cid[i] = lutL[(n >> 9) * 8 + r]; }
#pragma unroll
      for (int i = 0; i < 4; ++i) sl[i] = atomicAdd(&cntL[cid[i]], 1u);
#pragma unroll
      for (int i = 0; i < 4; ++i) {
        const unsigned ln = ((unsigned)d4[i] >> 3) & 63;
        if (sl[i] < CAP) segL[cid[i] * CAP + sl[i]] = (ln << 16) | (unsigned)s4[i];
        else { const unsigned o = atomicAdd(&cntL[255], 1u); ((unsigned*)(ws + WS_O1))[b * 2048 + o] = ((cid[i] * 64 + ln) << 14) | (unsigned)s4[i]; }
      }
    }
    __syncthreads();
    STAMPG(0, 2);
#pragma unroll
    for (int i = 0; i < 4; ++i) {
      const int o = tid + 512 * i;
      if (o < 2000) { const int c = o / 10, u = o - c * 10; *(v4u*)(ws + WS_SEG + (size_t)c * SEGB + b * (CAP * 4) + u * 16) = ((const v4u*)segL)[o]; }
    }
    if (tid == 0) ((unsigned*)(ws + WS_O1C))[b] = cntL[255];
  } else if (b < G1 + NODEWG) {
    const int nb = b - G1, n0 = nb * 128;
    char* xL = lds;
    u8* rowL = (u8*)(lds + 128 * 272);
    float* dL = (float*)(lds + 128 * 272 + 128);
    const __amdgpu_buffer_rsrc_t rx = mkrsrc(x, NN * 256);
    v4u xv[4];
#pragma unroll
    for (int i = 0; i < 4; ++i) xv[i] = __builtin_amdgcn_raw_buffer_load_b128(rx, n0 * 256 + (tid + 512 * i) * 16, 0, 0);
    if (tid < 128) {
      const int n = n0 + tid;
      const v4u d = __builtin_amdgcn_raw_buffer_load_b128(rdis, (n >> 3) * 16, 0, 0);
      const v4u k = keys8(d);
      const int e = tid & 7;
      const unsigned kw = (e >> 1) == 0 ? k[0] : (e >> 1) == 1 ? k[1] : (e >> 1) == 2 ? k[2] : k[3];
      const unsigned dw = (e >> 1) == 0 ? d[0] : (e >> 1) == 1 ? d[1] : (e >> 1) == 2 ? d[2] : d[3];
      const unsigned r = rank8(k, (e & 1) ? kw >> 16 : kw & 0xffff);
      const float dv = (e & 1) ? bfhi(dw) : bflo(dw);
      const unsigned pos = chunk_of(n >> 9, r) * 64 + ((n >> 3) & 63);
      ((u16*)(ws + WS_POS))[n] = (u16)pos;
      ((u16*)(ws + WS_INV))[pos] = n < NN ? (u16)n : (u16)0xFFFF;
      ((float*)(ws + WS_PDIS))[pos] = dv;
      rowL[r * 16 + (tid >> 3)] = (u8)tid;
      dL[tid] = dv;
    }
    {
      const v4u s = {~0u, ~0u, ~0u, ~0u};
      for (int o = nb * 512 + tid; o < HOPB / 16; o += NODEWG * 512) *(v4u*)(ws + WS_B + HOPB + (size_t)o * 16) = s;
    }
#pragma unroll
    for (int i = 0; i < 4; ++i) { const int o = tid + 512 * i; *(v4u*)(xL + (o >> 4) * 272 + (o & 15) * 16) = xv[i]; }
    __syncthreads();
    const int u = tid & 7, r = (tid >> 6), sq = (tid >> 3) & 7;
    const int ra = rowL[r * 16 + 2 * u], rb = rowL[r * 16 + 2 * u + 1];
    const float da = dL[ra], db = dL[rb];
    const unsigned pbyte = (chunk_of(n0 >> 9, r) * 64 + ((n0 >> 3) & 63) + 2 * u) * 8;
#pragma unroll
    for (int i = 0; i < 4; ++i) {
      const int s = sq + 8 * i;
      const v2u a = *(const v2u*)(xL + ra * 272 + s * 8), c = *(const v2u*)(xL + rb * 272 + s * 8);
      *(v4u*)(ws + WS_B + (size_t)s * SLB + pbyte) = (v4u){a[0], a[1], c[0], c[1]};
      *(v4u*)(ws + WS_B + (size_t)8 * HOPB + (size_t)s * SLB + pbyte) =
          (v4u){pkbf(bflo(a[0]) * da, bfhi(a[0]) * da), pkbf(bflo(a[1]) * da, bfhi(a[1]) * da), pkbf(bflo(c[0]) * db, bfhi(c[0]) * db), pkbf(bflo(c[1]) * db, bfhi(c[1]) * db)};
    }
  } else {
    const int which = b - G1 - NODEWG, v = tid >> 6, lane = tid & 63, g = lane >> 4, c = lane & 15;
    const int e = 32 * (v >> 1) + 8 * (c >> 2) + 4 * (v & 1) + (c & 3);
    const u16* wa = which == 0 ? wq : wv;
    v8bf bf[4];
#pragma unroll
    for (int ks = 0; ks < 4; ++ks) {
      if (which == 0) bf[ks] = *(const v8bf*)(wk + e * 128 + 32 * ks + 8 * g);
      else {
        v4u t;
#pragma unroll
        for (int i = 0; i < 4; ++i) t[i] = (unsigned)wo[(32 * ks + 8 * g + 2 * i) * 128 + e] | ((unsigned)wo[(32 * ks + 8 * g + 2 * i + 1) * 128 + e] << 16);
        bf[ks] = __builtin_bit_cast(v8bf, t);
      }
    }
    v4f acc[8];
    float cs = 0.f;
#pragma unroll
    for (int rt = 0; rt < 8; ++rt) {
      acc[rt] = (v4f){0, 0, 0, 0};
#pragma unroll
      for (int ks = 0; ks < 4; ++ks) acc[rt] = __builtin_amdgcn_mfma_f32_16x16x32_bf16(*(const v8bf*)(wa + (16 * rt + c) * 128 + 32 * ks + 8 * g), bf[ks], acc[rt], 0, 0, 0);
      cs += acc[rt][0] + acc[rt][1] + acc[rt][2] + acc[rt][3];
    }
    cs = add32(add16(cs)) * (1.f / 128.f);
#pragma unroll
    for (int rt = 0; rt < 8; ++rt) acc[rt] -= cs;
    if (which == 0) {
      float* rs = (float*)lds;
#pragma unroll
      for (int rt = 0; rt < 8; ++rt)
#pragma unroll
        for (int r = 0; r < 4; ++r) {
          float t = acc[rt][r];
          t += dppf<DPP_X1>(t); t += dppf<DPP_X2>(t); t += dppf<DPP_HM>(t); t += dppf<DPP_RM>(t);
          if (c == 0) rs[v * 128 + 16 * rt + 4 * g + r] = t;
        }
      __syncthreads();
      const float sc = 1.4426950408889634f * 0.08838834764831845f;
#pragma unroll
      for (int rt = 0; rt < 8; ++rt) {
        v4f m = {0, 0, 0, 0};
#pragma unroll
        for (int w = 0; w < 8; ++w) m += *(const v4f*)(rs + w * 128 + 16 * rt + 4 * g);
        acc[rt] = (acc[rt] - m * (1.f / 128.f)) * sc;
      }
    }
#pragma unroll
    for (int rt = 0; rt < 8; ++rt) {
      const int ks = rt >> 1, gg = 2 * (rt & 1) + (g >> 1);
      *(v2u*)(ws + WS_WF + which * 32768 + ((v * 4 + ks) * 64 + 16 * gg + c) * 16 + 8 * (g & 1)) = (v2u){pkbf(acc[rt][0], acc[rt][1]), pkbf(acc[rt][2], acc[rt][3])};
    }
  }
  STAMPG(0, 3);
}

constexpr int L2_POS = 0, L2_ENT = 25600, L2_CNT = L2_ENT + 16384, L2_EXT = L2_CNT + 8192, L2_MSK = L2_EXT + 4096, L2_OUT = L2_MSK + 1024, L2_PICK = L2_OUT + 8192, K2_LDS = L2_PICK + 8192;
__global__ void __launch_bounds__(1024, 4) k_lists(char* __restrict__ ws) {
  extern __shared__ __attribute__((aligned(16))) char lds[];
  const int tid = threadIdx.x, ch = blockIdx.x, lane = tid & 63, wave = __builtin_amdgcn_readfirstlane(tid >> 6);
  STAMPG(1, 0);
  u16* posL = (u16*)(lds + L2_POS);
  unsigned* entL = (unsigned*)(lds + L2_ENT);
  unsigned* cntL = (unsigned*)(lds + L2_CNT);
  u8* extL = (u8*)(lds + L2_EXT);
  unsigned* mskL = (unsigned*)(lds + L2_MSK);
  u16* outL = (u16*)(lds + L2_OUT);
  unsigned* pickL = (unsigned*)(lds + L2_PICK);
  unsigned* o2 = (unsigned*)(ws + WS_O2 + (size_t)ch * SEGB);
  v4u e4[2], p4[2];
  const v4u* seg = (const v4u*)(ws + WS_SEG + (size_t)ch * SEGB);
  e4[0] = seg[tid];
  e4[1] = tid < SEGB / 16 - 1024 ? seg[tid + 1024] : (v4u){~0u, ~0u, ~0u, ~0u};
  p4[0] = ((const v4u*)(ws + WS_POS))[tid];
  if (tid < 1568 - 1024) p4[1] = ((const v4u*)(ws + WS_POS))[tid + 1024];
  const unsigned o1c = tid < G1 ? ((const unsigned*)(ws + WS_O1C))[tid] : 0u;
  if (tid < (8192 + 4096 + 1024) / 16) ((v4u*)(lds + L2_CNT))[tid] = (v4u){0, 0, 0, 0};
  if (tid >= 512) ((v4u*)(lds + L2_OUT))[tid - 512] = (v4u){~0u, ~0u, ~0u, ~0u};
  __syncthreads();
  {
    unsigned off[8], tot = 0;
#pragma unroll
    for (int i = 0; i < 8; ++i) {
      const u64 m = __ballot(e4[i >> 2][i & 3] != ~0u);
      off[i] = tot + __builtin_amdgcn_mbcnt_hi((unsigned)(m >> 32), __builtin_amdgcn_mbcnt_lo((unsigned)m, 0));
      tot += __builtin_popcountll(m);
    }
    unsigned base = 0;
    if (lane == 0) base = atomicAdd(&mskL[192], tot);
    base = __builtin_amdgcn_readfirstlane(base);
    ((v4u*)posL)[tid] = p4[0];
    if (tid < 1568 - 1024) ((v4u*)posL)[tid + 1024] = p4[1];
    if (o1c) mskL[195] = 1;
    bool late = false;
#pragma unroll
    for (int i = 0; i < 8; ++i) {
      const unsigned e = e4[i >> 2][i & 3];
      if (e != ~0u) { if (base + off[i] < 4096) entL[base + off[i]] = e; else late = true; }
    }
    __syncthreads();
    if (late) {
#pragma unroll
      for (int i = 0; i < 8; ++i) {
        const unsigned e = e4[i >> 2][i & 3];
        if (e != ~0u && base + off[i] >= 4096) o2[atomicAdd(&mskL[193], 1u)] = (e & 0x3F0000u) | posL[e & 0xFFFF];
      }
    }
  }
  STAMPG(1, 1);
  const unsigned nent = min(mskL[192], 4096u);
  unsigned sp[4], rk[4];
#pragma unroll
  for (int i = 0; i < 4; ++i) {
    rk[i] = 0; sp[i] = 0;
    if (tid + 1024 * i < nent) {
      const unsigned e = entL[tid + 1024 * i];
      const unsigned p = posL[e & 0xFFFF], ln = e >> 16, d = (ln ^ p) & 31;
      sp[i] = (e & 0x3F0000u) | p;
      const unsigned r = atomicAdd(&cntL[d * 64 + ln], 1u);
      rk[i] = r;
      if (r == 0) {
        outL[((d >> 3) * 64 + ln) * 8 + (d & 7)] = (u16)p;
        atomicOr(&mskL[ln], 1u << d);
        atomicOr(&mskL[64 + (ln & 32) + (p & 31)], 1u << d);
      } else if (r <= 2) extL[ln * 64 + d * 2 + r - 1] = 1;
    }
  }
  __syncthreads();
  STAMPG(1, 2);
  if (wave == 0) {
    unsigned lf = ~mskL[lane], bf = ~mskL[64 + lane];
    v4u ex[4];
#pragma unroll
    for (int i = 0; i < 4; ++i) ex[i] = ((const v4u*)extL)[lane * 4 + i];
    int d = 0;
#pragma unroll
    for (int i = 0; i < 32; ++i) {
      if (i) {
        const int t = __builtin_ctz(i);
        if (t == 0) { bf = dppu<DPP_X1>(bf); d ^= 1; }
        else if (t == 1) { bf = dppu<DPP_X2>(bf); d ^= 2; }
        else if (t == 2) { bf = dppu<DPP_HM>(bf); d ^= 7; }
        else if (t == 3) { bf = dppu<DPP_RM>(bf); d ^= 15; }
        else { bf = (unsigned)__builtin_amdgcn_ds_swizzle((int)bf, 0x401F); d ^= 16; }
      }
      const unsigned w = ex[d >> 3][(d >> 1) & 3];
      const unsigned a1 = (w >> (16 * (d & 1))) & 0xff, a2 = (w >> (16 * (d & 1) + 8)) & 0xff;
      const unsigned f0 = lf & bf;
      unsigned f = f0 & (f0 - a1);
      f &= f - a2;
      const unsigned p = f0 ^ f;
      lf ^= p; bf ^= p;
      pickL[d * 64 + lane] = p;
    }
    mskL[lane] = lf;
  } else if (wave == 1) {
    unsigned dg = 0;
#pragma unroll
    for (int d = 0; d < 32; ++d) dg += cntL[d * 64 + lane];
    dg = max(dg, dppu<DPP_X1>(dg)); dg = max(dg, dppu<DPP_X2>(dg)); dg = max(dg, dppu<DPP_HM>(dg)); dg = max(dg, dppu<DPP_RM>(dg));
    const unsigned m = max(max((unsigned)__builtin_amdgcn_readlane(dg, 0), (unsigned)__builtin_amdgcn_readlane(dg, 16)), max((unsigned)__builtin_amdgcn_readlane(dg, 32), (unsigned)__builtin_amdgcn_readlane(dg, 48)));
    if (lane == 0) mskL[194] = min(max((m + 3) & ~3u, 32u), 64u);
  }
  __syncthreads();
  STAMPG(1, 3);
  const unsigned L = mskL[194];
#pragma unroll
  for (int i = 0; i < 4; ++i) {
    if (rk[i]) {
      const unsigned p = sp[i] & 0xFFFF, ln = sp[i] >> 16, d = (ln ^ p) & 31;
      unsigned m = rk[i] <= 2 ? pickL[d * 64 + ln] : 0u;
      if (rk[i] == 2) m &= m - 1;
      int c;
      if (m) c = __builtin_ctz(m);
      else {
        unsigned k = atomicAdd(&mskL[128 + ln], 1u);
        if (k < L - 32) c = L - 1 - k;
        else {
          k -= L - 32;
          unsigned fr = mskL[ln];
          while (k && fr) { fr ^= 0x80000000u >> __builtin_clz(fr); --k; }
          c = fr ? 31 - __builtin_clz(fr) : -1;
        }
      }
      if (c >= 0) outL[((c >> 3) * 64 + ln) * 8 + (c & 7)] = (u16)p;
      else o2[atomicAdd(&mskL[193], 1u)] = sp[i];
    }
  }
  __syncthreads();
  STAMPG(1, 4);
  {
    v2u v = *(const v2u*)(outL + ((wave >> 1) * 64 + lane) * 8 + 4 * (wave & 1));
    unsigned h[4] = {v[0] & 0xFFFF, v[0] >> 16, v[1] & 0xFFFF, v[1] >> 16};
#pragma unroll
    for (int i = 0; i < 4; ++i) {
      unsigned u = h[i] == 0xFFFF ? 0u : 1u << (h[i] & 31);
      u |= dppu<DPP_X1>(u); u |= dppu<DPP_X2>(u); u |= dppu<DPP_HM>(u); u |= dppu<DPP_RM>(u); u = or16(u);
      if (h[i] == 0xFFFF) h[i] = NPOS + __builtin_ctz(~u);
    }
    *(v2u*)(ws + WS_LIST + (size_t)ch * 8192 + ((wave >> 1) * 64 + lane) * 16 + 8 * (wave & 1)) = (v2u){h[0] | (h[1] << 16), h[2] | (h[3] << 16)};
    if (tid == 0) *(v4u*)(ws + WS_META + ch * 16) = (v4u){L >> 1, mskL[193], mskL[195], 0u};
  }
  STAMPG(1, 5);
}

#define GATHER_ASM \
  "s_waitcnt lgkmcnt(0)\n" \
  "v_lshlrev_b32_sdwa v8, 3, %4 dst_sel:DWORD dst_unused:UNUSED_PAD src0_sel:DWORD src1_sel:WORD_0\n" \
  "v_lshlrev_b32_sdwa v10, 3, %4 dst_sel:DWORD dst_unused:UNUSED_PAD src0_sel:DWORD src1_sel:WORD_1\n" \
  "ds_read_b64 v[8:9], v8\n" \
  "ds_read_b64 v[10:11], v10\n" \
  "v_lshlrev_b32_sdwa v12, 3, %5 dst_sel:DWORD dst_unused:UNUSED_PAD src0_sel:DWORD src1_sel:WORD_0\n" \
  "v_lshlrev_b32_sdwa v14, 3, %5 dst_sel:DWORD dst_unused:UNUSED_PAD src0_sel:DWORD src1_sel:WORD_1\n" \
  "ds_read_b64 v[12:13], v12\n" \
  "ds_read_b64 v[14:15], v14\n" \
  "v_lshlrev_b32_sdwa v16, 3, %6 dst_sel:DWORD dst_unused:UNUSED_PAD src0_sel:DWORD src1_sel:WORD_0\n" \
  "v_lshlrev_b32_sdwa v18, 3, %6 dst_sel:DWORD dst_unused:UNUSED_PAD src0_sel:DWORD src1_sel:WORD_1\n" \
  "ds_read_b64 v[16:17], v16\n" \
  "ds_read_b64 v[18:19], v18\n" \
  "v_lshlrev_b32_sdwa v20, 3, %7 dst_sel:DWORD dst_unused:UNUSED_PAD src0_sel:DWORD src1_sel:WORD_0\n" \
  "v_lshlrev_b32_sdwa v22, 3, %7 dst_sel:DWORD dst_unused:UNUSED_PAD src0_sel:DWORD src1_sel:WORD_1\n" \
  "ds_read_b64 v[20:21], v20\n" \
  "ds_read_b64 v[22:23], v22\n" \
  "s_waitcnt lgkmcnt(4)\n" \
  "v_smfmac_f32_16x16x64_bf16 %0, %1, v[8:15], %2\n" \
  "v_lshlrev_b32_sdwa v8, 3, %8 dst_sel:DWORD dst_unused:UNUSED_PAD src0_sel:DWORD src1_sel:WORD_0\n" \
  "v_lshlrev_b32_sdwa v10, 3, %8 dst_sel:DWORD dst_unused:UNUSED_PAD src0_sel:DWORD src1_sel:WORD_1\n" \
  "ds_read_b64 v[8:9], v8\n" \
  "ds_read_b64 v[10:11], v10\n" \
  "v_lshlrev_b32_sdwa v12, 3, %9 dst_sel:DWORD dst_unused:UNUSED_PAD src0_sel:DWORD src1_sel:WORD_0\n" \
  "v_lshlrev_b32_sdwa v14, 3, %9 dst_sel:DWORD dst_unused:UNUSED_PAD src0_sel:DWORD src1_sel:WORD_1\n" \
  "ds_read_b64 v[12:13], v12\n" \
  "ds_read_b64 v[14:15], v14\n" \
  "s_waitcnt lgkmcnt(4)\n" \
  "v_smfmac_f32_16x16x64_bf16 %0, %1, v[16:23], %2\n" \
  "v_lshlrev_b32_sdwa v16, 3, %10 dst_sel:DWORD dst_unused:UNUSED_PAD src0_sel:DWORD src1_sel:WORD_0\n" \
  "v_lshlrev_b32_sdwa v18, 3, %10 dst_sel:DWORD dst_unused:UNUSED_PAD src0_sel:DWORD src1_sel:WORD_1\n" \
  "ds_read_b64 v[16:17], v16\n" \
  "ds_read_b64 v[18:19], v18\n" \
  "v_lshlrev_b32_sdwa v20, 3, %11 dst_sel:DWORD dst_unused:UNUSED_PAD src0_sel:DWORD src1_sel:WORD_0\n" \
  "v_lshlrev_b32_sdwa v22, 3, %11 dst_sel:DWORD dst_unused:UNUSED_PAD src0_sel:DWORD src1_sel:WORD_1\n" \
  "ds_read_b64 v[20:21], v20\n" \
  "ds_read_b64 v[22:23], v22\n" \
  "s_waitcnt lgkmcnt(4)\n" \
  "v_smfmac_f32_16x16x64_bf16 %0, %1, v[8:15], %2\n" \
  "v_lshlrev_b32_sdwa v8, 3, %12 dst_sel:DWORD dst_unused:UNUSED_PAD src0_sel:DWORD src1_sel:WORD_0\n" \
  "v_lshlrev_b32_sdwa v10, 3, %12 dst_sel:DWORD dst_unused:UNUSED_PAD src0_sel:DWORD src1_sel:WORD_1\n" \
  "ds_read_b64 v[8:9], v8\n" \
  "ds_read_b64 v[10:11], v10\n" \
  "v_lshlrev_b32_sdwa v12, 3, %13 dst_sel:DWORD dst_unused:UNUSED_PAD src0_sel:DWORD src1_sel:WORD_0\n" \
  "v_lshlrev_b32_sdwa v14, 3, %13 dst_sel:DWORD dst_unused:UNUSED_PAD src0_sel:DWORD src1_sel:WORD_1\n" \
  "ds_read_b64 v[12:13], v12\n" \
  "ds_read_b64 v[14:15], v14\n" \
  "s_waitcnt lgkmcnt(4)\n" \
  "v_smfmac_f32_16x16x64_bf16 %0, %1, v[16:23], %2\n" \
  "v_lshlrev_b32_sdwa v16, 3, %14 dst_sel:DWORD dst_unused:UNUSED_PAD src0_sel:DWORD src1_sel:WORD_0\n" \
  "v_lshlrev_b32_sdwa v18, 3, %14 dst_sel:DWORD dst_unused:UNUSED_PAD src0_sel:DWORD src1_sel:WORD_1\n" \
  "ds_read_b64 v[16:17], v16\n" \
  "ds_read_b64 v[18:19], v18\n" \
  "v_lshlrev_b32_sdwa v20, 3, %15 dst_sel:DWORD dst_unused:UNUSED_PAD src0_sel:DWORD src1_sel:WORD_0\n" \
  "v_lshlrev_b32_sdwa v22, 3, %15 dst_sel:DWORD dst_unused:UNUSED_PAD src0_sel:DWORD src1_sel:WORD_1\n" \
  "ds_read_b64 v[20:21], v20\n" \
  "ds_read_b64 v[22:23], v22\n" \
  "s_waitcnt lgkmcnt(4)\n" \
  "v_smfmac_f32_16x16x64_bf16 %0, %1, v[8:15], %2\n" \
  "v_lshlrev_b32_sdwa v8, 3, %16 dst_sel:DWORD dst_unused:UNUSED_PAD src0_sel:DWORD src1_sel:WORD_0\n" \
  "v_lshlrev_b32_sdwa v10, 3, %16 dst_sel:DWORD dst_unused:UNUSED_PAD src0_sel:DWORD src1_sel:WORD_1\n" \
  "ds_read_b64 v[8:9], v8\n" \
  "ds_read_b64 v[10:11], v10\n" \
  "v_lshlrev_b32_sdwa v12, 3, %17 dst_sel:DWORD dst_unused:UNUSED_PAD src0_sel:DWORD src1_sel:WORD_0\n" \
  "v_lshlrev_b32_sdwa v14, 3, %17 dst_sel:DWORD dst_unused:UNUSED_PAD src0_sel:DWORD src1_sel:WORD_1\n" \
  "ds_read_b64 v[12:13], v12\n" \
  "ds_read_b64 v[14:15], v14\n" \
  "s_waitcnt lgkmcnt(4)\n" \
  "v_smfmac_f32_16x16x64_bf16 %0, %1, v[16:23], %2\n" \
  "v_lshlrev_b32_sdwa v16, 3, %18 dst_sel:DWORD dst_unused:UNUSED_PAD src0_sel:DWORD src1_sel:WORD_0\n" \
  "v_lshlrev_b32_sdwa v18, 3, %18 dst_sel:DWORD dst_unused:UNUSED_PAD src0_sel:DWORD src1_sel:WORD_1\n" \
  "ds_read_b64 v[16:17], v16\n" \
  "ds_read_b64 v[18:19], v18\n" \
  "v_lshlrev_b32_sdwa v20, 3, %19 dst_sel:DWORD dst_unused:UNUSED_PAD src0_sel:DWORD src1_sel:WORD_0\n" \
  "v_lshlrev_b32_sdwa v22, 3, %19 dst_sel:DWORD dst_unused:UNUSED_PAD src0_sel:DWORD src1_sel:WORD_1\n" \
  "ds_read_b64 v[20:21], v20\n" \
  "ds_read_b64 v[22:23], v22\n" \
  "s_cmp_le_u32 %3, 16\n" \
  "s_cbranch_scc1 Lgt%=\n" \
  "s_waitcnt lgkmcnt(4)\n" \
  "v_smfmac_f32_16x16x64_bf16 %0, %1, v[8:15], %2\n" \
  "v_lshlrev_b32_sdwa v8, 3, %20 dst_sel:DWORD dst_unused:UNUSED_PAD src0_sel:DWORD src1_sel:WORD_0\n" \
  "v_lshlrev_b32_sdwa v10, 3, %20 dst_sel:DWORD dst_unused:UNUSED_PAD src0_sel:DWORD src1_sel:WORD_1\n" \
  "ds_read_b64 v[8:9], v8\n" \
  "ds_read_b64 v[10:11], v10\n" \
  "v_lshlrev_b32_sdwa v12, 3, %21 dst_sel:DWORD dst_unused:UNUSED_PAD src0_sel:DWORD src1_sel:WORD_0\n" \
  "v_lshlrev_b32_sdwa v14, 3, %21 dst_sel:DWORD dst_unused:UNUSED_PAD src0_sel:DWORD src1_sel:WORD_1\n" \
  "ds_read_b64 v[12:13], v12\n" \
  "ds_read_b64 v[14:15], v14\n" \
  "s_cmp_le_u32 %3, 18\n" \
  "s_cbranch_scc1 Lgt%=\n" \
  "s_waitcnt lgkmcnt(4)\n" \
  "v_smfmac_f32_16x16x64_bf16 %0, %1, v[16:23], %2\n" \
  "v_lshlrev_b32_sdwa v16, 3, %22 dst_sel:DWORD dst_unused:UNUSED_PAD src0_sel:DWORD src1_sel:WORD_0\n" \
  "v_lshlrev_b32_sdwa v18, 3, %22 dst_sel:DWORD dst_unused:UNUSED_PAD src0_sel:DWORD src1_sel:WORD_1\n" \
  "ds_read_b64 v[16:17], v16\n" \
  "ds_read_b64 v[18:19], v18\n" \
  "v_lshlrev_b32_sdwa v20, 3, %23 dst_sel:DWORD dst_unused:UNUSED_PAD src0_sel:DWORD src1_sel:WORD_0\n" \
  "v_lshlrev_b32_sdwa v22, 3, %23 dst_sel:DWORD dst_unused:UNUSED_PAD src0_sel:DWORD src1_sel:WORD_1\n" \
  "ds_read_b64 v[20:21], v20\n" \
  "ds_read_b64 v[22:23], v22\n" \
  "s_cmp_le_u32 %3, 20\n" \
  "s_cbranch_scc1 Lgt%=\n" \
  "s_waitcnt lgkmcnt(4)\n" \
  "v_smfmac_f32_16x16x64_bf16 %0, %1, v[8:15], %2\n" \
  "v_lshlrev_b32_sdwa v8, 3, %24 dst_sel:DWORD dst_unused:UNUSED_PAD src0_sel:DWORD src1_sel:WORD_0\n" \
  "v_lshlrev_b32_sdwa v10, 3, %24 dst_sel:DWORD dst_unused:UNUSED_PAD src0_sel:DWORD src1_sel:WORD_1\n" \
  "ds_read_b64 v[8:9], v8\n" \
  "ds_read_b64 v[10:11], v10\n" \
  "v_lshlrev_b32_sdwa v12, 3, %25 dst_sel:DWORD dst_unused:UNUSED_PAD src0_sel:DWORD src1_sel:WORD_0\n" \
  "v_lshlrev_b32_sdwa v14, 3, %25 dst_sel:DWORD dst_unused:UNUSED_PAD src0_sel:DWORD src1_sel:WORD_1\n" \
  "ds_read_b64 v[12:13], v12\n" \
  "ds_read_b64 v[14:15], v14\n" \
  "s_cmp_le_u32 %3, 22\n" \
  "s_cbranch_scc1 Lgt%=\n" \
  "s_waitcnt lgkmcnt(4)\n" \
  "v_smfmac_f32_16x16x64_bf16 %0, %1, v[16:23], %2\n" \
  "v_lshlrev_b32_sdwa v16, 3, %26 dst_sel:DWORD dst_unused:UNUSED_PAD src0_sel:DWORD src1_sel:WORD_0\n" \
  "v_lshlrev_b32_sdwa v18, 3, %26 dst_sel:DWORD dst_unused:UNUSED_PAD src0_sel:DWORD src1_sel:WORD_1\n" \
  "ds_read_b64 v[16:17], v16\n" \
  "ds_read_b64 v[18:19], v18\n" \
  "v_lshlrev_b32_sdwa v20, 3, %27 dst_sel:DWORD dst_unused:UNUSED_PAD src0_sel:DWORD src1_sel:WORD_0\n" \
  "v_lshlrev_b32_sdwa v22, 3, %27 dst_sel:DWORD dst_unused:UNUSED_PAD src0_sel:DWORD src1_sel:WORD_1\n" \
  "ds_read_b64 v[20:21], v20\n" \
  "ds_read_b64 v[22:23], v22\n" \
  "s_cmp_le_u32 %3, 24\n" \
  "s_cbranch_scc1 Lgt%=\n" \
  "s_waitcnt lgkmcnt(4)\n" \
  "v_smfmac_f32_16x16x64_bf16 %0, %1, v[8:15], %2\n" \
  "v_lshlrev_b32_sdwa v8, 3, %28 dst_sel:DWORD dst_unused:UNUSED_PAD src0_sel:DWORD src1_sel:WORD_0\n" \
  "v_lshlrev_b32_sdwa v10, 3, %28 dst_sel:DWORD dst_unused:UNUSED_PAD src0_sel:DWORD src1_sel:WORD_1\n" \
  "ds_read_b64 v[8:9], v8\n" \
  "ds_read_b64 v[10:11], v10\n" \
  "v_lshlrev_b32_sdwa v12, 3, %29 dst_sel:DWORD dst_unused:UNUSED_PAD src0_sel:DWORD src1_sel:WORD_0\n" \
  "v_lshlrev_b32_sdwa v14, 3, %29 dst_sel:DWORD dst_unused:UNUSED_PAD src0_sel:DWORD src1_sel:WORD_1\n" \
  "ds_read_b64 v[12:13], v12\n" \
  "ds_read_b64 v[14:15], v14\n" \
  "s_cmp_le_u32 %3, 26\n" \
  "s_cbranch_scc1 Lgt%=\n" \
  "s_waitcnt lgkmcnt(4)\n" \
  "v_smfmac_f32_16x16x64_bf16 %0, %1, v[16:23], %2\n" \
  "v_lshlrev_b32_sdwa v16, 3, %30 dst_sel:DWORD dst_unused:UNUSED_PAD src0_sel:DWORD src1_sel:WORD_0\n" \
  "v_lshlrev_b32_sdwa v18, 3, %30 dst_sel:DWORD dst_unused:UNUSED_PAD src0_sel:DWORD src1_sel:WORD_1\n" \
  "ds_read_b64 v[16:17], v16\n" \
  "ds_read_b64 v[18:19], v18\n" \
  "v_lshlrev_b32_sdwa v20, 3, %31 dst_sel:DWORD dst_unused:UNUSED_PAD src0_sel:DWORD src1_sel:WORD_0\n" \
  "v_lshlrev_b32_sdwa v22, 3, %31 dst_sel:DWORD dst_unused:UNUSED_PAD src0_sel:DWORD src1_sel:WORD_1\n" \
  "ds_read_b64 v[20:21], v20\n" \
  "ds_read_b64 v[22:23], v22\n" \
  "s_cmp_le_u32 %3, 28\n" \
  "s_cbranch_scc1 Lgt%=\n" \
  "s_waitcnt lgkmcnt(4)\n" \
  "v_smfmac_f32_16x16x64_bf16 %0, %1, v[8:15], %2\n" \
  "v_lshlrev_b32_sdwa v8, 3, %32 dst_sel:DWORD dst_unused:UNUSED_PAD src0_sel:DWORD src1_sel:WORD_0\n" \
  "v_lshlrev_b32_sdwa v10, 3, %32 dst_sel:DWORD dst_unused:UNUSED_PAD src0_sel:DWORD src1_sel:WORD_1\n" \
  "ds_read_b64 v[8:9], v8\n" \
  "ds_read_b64 v[10:11], v10\n" \
  "v_lshlrev_b32_sdwa v12, 3, %33 dst_sel:DWORD dst_unused:UNUSED_PAD src0_sel:DWORD src1_sel:WORD_0\n" \
  "v_lshlrev_b32_sdwa v14, 3, %33 dst_sel:DWORD dst_unused:UNUSED_PAD src0_sel:DWORD src1_sel:WORD_1\n" \
  "ds_read_b64 v[12:13], v12\n" \
  "ds_read_b64 v[14:15], v14\n" \
  "s_cmp_le_u32 %3, 30\n" \
  "s_cbranch_scc1 Lgt%=\n" \
  "s_waitcnt lgkmcnt(4)\n" \
  "v_smfmac_f32_16x16x64_bf16 %0, %1, v[16:23], %2\n" \
  "v_lshlrev_b32_sdwa v16, 3, %34 dst_sel:DWORD dst_unused:UNUSED_PAD src0_sel:DWORD src1_sel:WORD_0\n" \
  "v_lshlrev_b32_sdwa v18, 3, %34 dst_sel:DWORD dst_unused:UNUSED_PAD src0_sel:DWORD src1_sel:WORD_1\n" \
  "ds_read_b64 v[16:17], v16\n" \
  "ds_read_b64 v[18:19], v18\n" \
  "v_lshlrev_b32_sdwa v20, 3, %35 dst_sel:DWORD dst_unused:UNUSED_PAD src0_sel:DWORD src1_sel:WORD_0\n" \
  "v_lshlrev_b32_sdwa v22, 3, %35 dst_sel:DWORD dst_unused:UNUSED_PAD src0_sel:DWORD src1_sel:WORD_1\n" \
  "ds_read_b64 v[20:21], v20\n" \
  "ds_read_b64 v[22:23], v22\n" \
  "Lgt%=:\n" \
  "s_waitcnt lgkmcnt(0)\n" \
  "v_smfmac_f32_16x16x64_bf16 %0, %1, v[8:15], %2\n" \
  "v_smfmac_f32_16x16x64_bf16 %0, %1, v[16:23], %2\n" \
  "s_nop 7\n" \
  ""
#define GATHER_CLOB "v8", "v9", "v10", "v11", "v12", "v13", "v14", "v15", "v16", "v17", "v18", "v19", "v20", "v21", "v22", "v23", "scc"

DEVI v4f gather(const v4u (&L)[8], int n, v8bf sel, int sidx) {
  v4f acc = {0.f, 0.f, 0.f, 0.f};
  asm volatile(GATHER_ASM : "+v"(acc) : "v"(sel), "v"(sidx), "s"(n),
    "v"(L[0][0]), "v"(L[0][1]), "v"(L[0][2]), "v"(L[0][3]), "v"(L[1][0]), "v"(L[1][1]), "v"(L[1][2]), "v"(L[1][3]),
    "v"(L[2][0]), "v"(L[2][1]), "v"(L[2][2]), "v"(L[2][3]), "v"(L[3][0]), "v"(L[3][1]), "v"(L[3][2]), "v"(L[3][3]),
    "v"(L[4][0]), "v"(L[4][1]), "v"(L[4][2]), "v"(L[4][3]), "v"(L[5][0]), "v"(L[5][1]), "v"(L[5][2]), "v"(L[5][3]),
    "v"(L[6][0]), "v"(L[6][1]), "v"(L[6][2]), "v"(L[6][3]), "v"(L[7][0]), "v"(L[7][1]), "v"(L[7][2]), "v"(L[7][3])
    : GATHER_CLOB, "memory");
  return acc;
}
DEVI v4f slow_edges(v4f acc, const char* ws, int ch, int n2, int any1, int lane) {
  const unsigned* o2 = (const unsigned*)(ws + WS_O2 + (size_t)ch * SEGB);
  const int lists = any1 ? G1 : 0;
  for (int li = -1; li < lists; ++li) {
    const int cnt = li < 0 ? n2 : (int)((const unsigned*)(ws + WS_O1C))[li];
    for (int base = 0; base < cnt; base += 64) {
      bool ok = base + lane < cnt;
      unsigned tl = 0, p = NPOS;
      if (ok) {
        if (li < 0) { const unsigned e = o2[base + lane]; tl = e >> 16; p = e & 0xFFFF; }
        else { const unsigned e = ((const unsigned*)(ws + WS_O1))[li * 2048 + base + lane]; ok = (int)(e >> 20) == ch; tl = (e >> 14) & 63; p = ((const u16*)(ws + WS_POS))[e & 0x3FFF]; }
      }
      const v2u val = *(LAS v2u*)(p * 8);
      u64 m = __ballot(ok);
      while (m) {
        const int i = __builtin_ctzll(m); m &= m - 1;
        const unsigned t = __builtin_amdgcn_readlane(tl, i), a = __builtin_amdgcn_readlane(val[0], i), c = __builtin_amdgcn_readlane(val[1], i);
        if ((unsigned)lane == t) { acc[0] += bflo(a); acc[1] += bfhi(a); acc[2] += bflo(c); acc[3] += bfhi(c); }
      }
    }
  }
  return acc;
}
DEVI unsigned pksafe(float a, float b) { return min(pkbf(a, b), 0xFFFFFFFEu); }
DEVI void publish(v4f a, float d2, __amdgpu_buffer_rsrc_t rs, int soff, int lane) {
  const unsigned x0 = pksafe(a[0] * d2, a[1] * d2), x1 = pksafe(a[2] * d2, a[3] * d2);
  const unsigned y0 = dppu<0xF5>(x0), y1 = dppu<0xF5>(x1);
  if (!(lane & 1)) __builtin_amdgcn_raw_buffer_store_b128((v4u){x0, x1, y0, y1}, rs, lane * 8, soff, 16);
}
constexpr int AUX_SC1V = 16;
constexpr int K3_LDS = SLB + 256;
__global__ void __launch_bounds__(1024, 4) k_hops(char* ws) {
  extern __shared__ __attribute__((aligned(16))) char lds[];
  const int b = blockIdx.x, slice = (b & 7) * 4 + (b >> 6), part = (b >> 3) & 7;
  const int wave = __builtin_amdgcn_readfirstlane(threadIdx.x >> 6), lane = threadIdx.x & 63;
  STAMPG(2, 0);
  const int c0 = part * 25 + wave, c1 = c0 + 16;
  const bool has1 = wave < 9;
  const __amdgpu_buffer_rsrc_t rs = mkrsrc(ws + WS_B, 9 * HOPB);
  const __amdgpu_buffer_rsrc_t rl = mkrsrc(ws + WS_LIST, NCH * 8192);
  const int l16 = lane * 16;
  constexpr int OOB = 0x40000000;
  v4u st[7];
#pragma unroll
  for (int i = 0; i < 7; ++i) st[i] = __builtin_amdgcn_raw_buffer_load_b128(rs, l16, (i < 6 || wave < 4) ? 8 * HOPB + slice * SLB + (wave + 16 * i) * 1024 : OOB, 0);
  const v4i m0 = *(const v4i*)(ws + WS_META + c0 * 16), m1 = *(const v4i*)(ws + WS_META + (has1 ? c1 : c0) * 16);
  const int n0 = m0[0], n1 = has1 ? m1[0] : 0;
  const bool slow0 = (m0[1] | m0[2]) != 0, slow1 = (m1[1] | m1[2]) != 0;
  v4u L0[8], L1[8];
#pragma unroll
  for (int i = 0; i < 8; ++i) L0[i] = __builtin_amdgcn_raw_buffer_load_b128(rl, l16, (i < 4 || 4 * i < n0) ? c0 * 8192 + i * 1024 : OOB, 0);
#pragma unroll
  for (int i = 0; i < 8; ++i) L1[i] = __builtin_amdgcn_raw_buffer_load_b128(rl, l16, (4 * i < n1) ? c1 * 8192 + i * 1024 : OOB, 0);
  float d0 = ((const float*)(ws + WS_PDIS))[c0 * 64 + lane];
  float d1 = (has1 && (wave < 8 || lane < 32)) ? ((const float*)(ws + WS_PDIS))[c1 * 64 + lane] : 0.f;
  d0 *= d0; d1 *= d1;
  v8bf sel;
  { const int g = lane >> 4, row = lane & 15;
#pragma unroll
    for (int j = 0; j < 8; ++j) sel[j] = (__bf16)(((row >> 2) == 2 * (g & 1) + (j >> 2) && (j & 1) == (row & 1)) ? 1.0f : 0.0f); }
  const int sidx = (lane & 2) ? 0xEEEE : 0x4444;
  const int lw = wave * 1024 + l16;
  if (threadIdx.x < 16) *(LAS v4u*)(SLB + l16) = (v4u){0, 0, 0, 0};
#pragma unroll
  for (int i = 0; i < 7; ++i) if (i < 6 || wave < 4) *(LAS v4u*)(lw + i * 16384) = st[i];
  __syncthreads();
  STAMPG(2, 1);
#pragma unroll 1
  for (int k = 1; k <= 7; ++k) {
    const int hb = k * HOPB + slice * SLB;
    if (k < 6 && (lane < 32 || has1))
      __builtin_amdgcn_raw_buffer_store_b128((v4u){~0u, ~0u, ~0u, ~0u}, rs, l16 + (lane < 32 ? 0 : 16 * 512 - 512), hb + HOPB + c0 * 512, 16);
    {
      v4f a = gather(L0, n0, sel, sidx);
      if (slow0) a = slow_edges(a, ws, c0, m0[1], m0[2], lane);
      asm volatile("s_waitcnt vmcnt(0)" ::: "memory");
      publish(a, d0, rs, hb + c0 * 512, lane);
    }
    if (has1) {
      v4f a = gather(L1, n1, sel, sidx);
      if (slow1) a = slow_edges(a, ws, c1, m1[1], m1[2], lane);
      publish(a, d1, rs, hb + c1 * 512, lane);
    }
    STAMPG(2, 4 * k - 2);
    if (k == 7) break;
    __builtin_amdgcn_s_barrier();
    STAMPG(2, 4 * k - 1);
    for (unsigned spins = 0; spins < SPINCAP; ++spins) {
      unsigned m = 0;
      asm volatile("" ::: "memory");
#pragma unroll
      for (int i = 0; i < 7; ++i) st[i] = __builtin_amdgcn_raw_buffer_load_b128(rs, l16, (i < 6 || wave < 4) ? hb + (wave + 16 * i) * 1024 : OOB, AUX_SC1V);
#pragma unroll
      for (int i = 0; i < 7; ++i) m = max(m, max(max(st[i][0], st[i][1]), max(st[i][2], st[i][3])));
      if (!__any(m == ~0u)) break;
      __builtin_amdgcn_s_sleep(4);
    }
    STAMPG(2, 4 * k);
#pragma unroll
    for (int i = 0; i < 7; ++i) if (i < 6 || wave < 4) *(LAS v4u*)(lw + i * 16384) = st[i];
    __syncthreads();
    STAMPG(2, 4 * k + 1);
  }
}

constexpr int T_W = 0, T_TOK = 56 * 1024, K4_LDS = T_TOK + 102400;
template <int NT>
DEVI void tf_tiles(const int tile0, const int pos0, const v8bf (&mreg)[8], const float (&wc)[32], const char* __restrict__ ws, u16* __restrict__ out, const int lane) {
  const int g = lane >> 4, c = lane & 15, hop = lane & 7, nb = (lane >> 3) & 1;
  v8bf go[NT][4];
  LAS char* const wb = ldsbase(T_W + lane * 16);
  LAS char* const ab0 = ldsbase(T_TOK + (g * 8 + hop) * 400 + tile0 * 16 + nb * 8);
  LAS char* const ab1 = ldsbase(T_TOK + (g * 8 + hop) * 400 + tile0 * 16 + nb * 8 + 4 * 12800);
  float cs[NT], rho[NT];
  unsigned node[NT];
#pragma unroll
  for (int t = 0; t < NT; ++t) {
    const int p = pos0 + 2 * (tile0 + t) + nb;
    const float dv = ((const float*)(ws + WS_PDIS))[p];
    node[t] = ((const u16*)(ws + WS_INV))[p];
    cs[t] = (hop == 0 || dv == 0.f) ? 1.f : 1.f / dv;
    float s1 = 0.f, s2 = 0.f;
#pragma unroll
    for (int j = 0; j < 4; ++j) {
      LAS char* const ab = (j < 2 ? ab0 : ab1) + t * 16 + (2 * (j & 1)) * 12800;
      const v4bf lo = *(LAS v4bf*)(ab), hi = *(LAS v4bf*)(ab + 12800);
      go[t][j] = __builtin_shufflevector(lo, hi, 0, 1, 2, 3, 4, 5, 6, 7);
#pragma unroll
      for (int i = 0; i < 4; ++i) {
        const v2bf pr = {go[t][j][2 * i], go[t][j][2 * i + 1]};
        s1 = __builtin_amdgcn_fdot2_f32_bf16(pr, (v2bf){(__bf16)1.0f, (__bf16)1.0f}, s1, false);
        s2 = __builtin_amdgcn_fdot2_f32_bf16(pr, pr, s2, false);
      }
    }
    s1 = add32(add16(s1)) * (1.f / 128.f); s2 = add32(add16(s2)) * (1.f / 128.f);
    const float var = fmaxf(s2 - s1 * s1, 0.f) * cs[t] * cs[t];
    rho[t] = cs[t] * __builtin_amdgcn_rsqf(var + 1e-5f);
  }
  v4f S[NT];
#pragma unroll
  for (int t = 0; t < NT; ++t) S[t] = (v4f){0, 0, 0, 0};
#pragma unroll
  for (int j = 0; j < 4; ++j) {
    v4f u[NT][2];
#pragma unroll
    for (int h = 0; h < 2; ++h) {
#pragma unroll
      for (int t = 0; t < NT; ++t) u[t][h] = (v4f){0, 0, 0, 0};
#pragma unroll
      for (int ks = 0; ks < 4; ++ks) {
        const int f = (2 * j + h) * 4 + ks;
        const v8bf w = f < 8 ? mreg[f & 7] : *(LAS v8bf*)(wb + (f - 8) * 1024);
#pragma unroll
        for (int t = 0; t < NT; ++t) u[t][h] = __builtin_amdgcn_mfma_f32_16x16x32_bf16(w, go[t][ks], u[t][h], 0, 0, 0);
      }
    }
#pragma unroll
    for (int t = 0; t < NT; ++t) {
      const v4u ub = {pkbf(u[t][0][0], u[t][0][1]), pkbf(u[t][0][2], u[t][0][3]), pkbf(u[t][1][0], u[t][1][1]), pkbf(u[t][1][2], u[t][1][3])};
      S[t] = __builtin_amdgcn_mfma_f32_16x16x32_bf16(go[t][j], __builtin_bit_cast(v8bf, ub), S[t], 0, 0, 0);
    }
  }
  v8bf pb[NT];
  const bool valid = (g >> 1) == nb;
  const unsigned dg0 = (c == 4 * g ? 0x3F80u : 0u) | (c == 4 * g + 1 ? 0x3F800000u : 0u), dg1 = (c == 4 * g + 2 ? 0x3F80u : 0u) | (c == 4 * g + 3 ? 0x3F800000u : 0u);
#pragma unroll
  for (int t = 0; t < NT; ++t) {
    float rl[4], sc[4];
#pragma unroll
    for (int r = 0; r < 4; ++r) { rl[r] = __shfl(rho[t], 4 * g + r); sc[r] = S[t][r] * rho[t] * rl[r]; }
    const float m = max16(fmaxf(fmaxf(sc[0], sc[1]), fmaxf(sc[2], sc[3])));
#pragma unroll
    for (int r = 0; r < 4; ++r) sc[r] = __builtin_amdgcn_exp2f(sc[r] - m);
    const float sum = add16(sc[0] + sc[1] + sc[2] + sc[3]);
    const float inv = __builtin_amdgcn_rcpf(sum * cs[t]);
    const unsigned p0 = pkbf(sc[0] * rl[0] * inv, sc[1] * rl[1] * inv), p1 = pkbf(sc[2] * rl[2] * inv, sc[3] * rl[3] * inv);
    pb[t] = __builtin_bit_cast(v8bf, (v4u){valid ? p0 : 0u, valid ? p1 : 0u, dg0, dg1});
  }
  v2u zb[NT][8];
  const int trb = T_TOK + ((lane & 3) * 8 + 4 * (g & 1) + ((lane >> 2) & 3)) * 400 + (g >> 1) * 8 + tile0 * 16;
  LAS char* const tr0 = ldsbase(trb);
  LAS char* const tr1 = ldsbase(trb + 4 * 12800);
#pragma unroll
  for (int T = 0; T < 8; ++T) {
    v4f z[NT];
#pragma unroll
    for (int t = 0; t < NT; ++t) z[t] = (v4f){0, 0, 0, 0};
#pragma unroll
    for (int ks = 0; ks < 4; ++ks) {
      const v8bf w = *(LAS v8bf*)(wb + (24 + T * 4 + ks) * 1024);
#pragma unroll
      for (int t = 0; t < NT; ++t) z[t] = __builtin_amdgcn_mfma_f32_16x16x32_bf16(go[t][ks], w, z[t], 0, 0, 0);
    }
#pragma unroll
    for (int t = 0; t < NT; ++t) zb[t][T] = (v2u){pkbf(z[t][0], z[t][1]), pkbf(z[t][2], z[t][3])};
  }
#pragma unroll
  for (int t = 0; t < NT; ++t) {
    __builtin_amdgcn_sched_barrier(0);
    v4f O[8];
#pragma unroll
    for (int T = 0; T < 8; ++T) {
      const v2u gt = __builtin_bit_cast(v2u, __builtin_amdgcn_ds_read_tr16_b64_v4bf16((LAS v4bf*)((T < 4 ? tr0 : tr1) + (T & 3) * 12800 + t * 16)));
      const v4u a = {zb[t][T][0], zb[t][T][1], gt[0], gt[1]};
      O[T] = __builtin_amdgcn_mfma_f32_16x16x32_bf16(__builtin_bit_cast(v8bf, a), pb[t], (v4f){0, 0, 0, 0}, 0, 0, 0);
    }
    float t1 = 0.f, t2 = 0.f, t3 = 0.f;
#pragma unroll
    for (int T = 0; T < 8; ++T)
#pragma unroll
      for (int r = 0; r < 4; ++r) { const float o = O[T][r]; t1 += o; t2 = fmaf(o, o, t2); t3 = fmaf(o, wc[T * 4 + r], t3); }
    t1 = add32(add16(t1)) * (1.f / 128.f); t2 = add32(add16(t2)) * (1.f / 128.f); t3 = add32(add16(t3));
    const float var = fmaxf(t2 - t1 * t1, 0.f) * cs[t] * cs[t];
    const float a = t3 * cs[t] * __builtin_amdgcn_rsqf(var + 1e-5f);
    float m = fmaxf(a, dppf<DPP_X1>(a)); m = fmaxf(m, dppf<DPP_X2>(m)); m = fmaxf(m, dppf<DPP_HM>(m));
    const float e = __builtin_amdgcn_exp2f(a - m);
    float s = e + dppf<DPP_X1>(e); s += dppf<DPP_X2>(s); s += dppf<DPP_HM>(s);
    const float gam = e * cs[t] * __builtin_amdgcn_rcpf(s);
    v4u res = {0, 0, 0, 0};
#pragma unroll
    for (int j = 0; j < 4; ++j) {
      float v[8];
#pragma unroll
      for (int i = 0; i < 8; ++i) {
        float q = O[2 * j + (i >> 2)][i & 3] * gam;
        q += dppf<DPP_X1>(q); q += dppf<DPP_X2>(q); q += dppf<DPP_HM>(q);
        v[i] = q;
      }
      if (hop == j) res = (v4u){pkbf(v[0], v[1]), pkbf(v[2], v[3]), pkbf(v[4], v[5]), pkbf(v[6], v[7])};
    }
    if (hop < 4 && node[t] < (unsigned)NN) *(v4u*)(out + node[t] * 128 + 32 * hop + 8 * g) = res;
  }
}
__global__ void __launch_bounds__(512, 2) k_tf(const char* __restrict__ ws, const u16* __restrict__ wr, u16* __restrict__ out) {
  extern __shared__ __attribute__((aligned(16))) char lds[];
  const int tid = threadIdx.x, lane = tid & 63, wave = __builtin_amdgcn_readfirstlane(tid >> 6);
  STAMPG(3, 0);
  const int part = blockIdx.x >> 5, idx = blockIdx.x & 31;
  const int t0 = idx * 782 / 32, nt = (idx + 1) * 782 / 32 - t0;
  const int pos0 = part * 1600 + 2 * t0;
#pragma unroll
  for (int i = 0; i < 13; ++i) {
    const int blk = wave + 8 * i;
    if (i < 12 || blk < 100) {
      const int xk = blk * 64 + lane, row = xk / 25, u = xk - row * 25;
      const int hop = row & 7, gg = (row >> 3) & 3, T = row >> 5, s = 8 * (T >> 1) + 2 * gg + (T & 1);
      __builtin_amdgcn_global_load_lds((const GAS unsigned*)(ws + WS_B + (size_t)hop * HOPB + s * SLB + pos0 * 8 + u * 16), (LAS unsigned*)(T_TOK + blk * 1024), 16, 0, 0);
    }
  }
#pragma unroll
  for (int i = 0; i < 7; ++i) {
    const int blk = wave + 8 * i;
    __builtin_amdgcn_global_load_lds((const GAS unsigned*)(ws + WS_WF + (8 + blk) * 1024 + lane * 16), (LAS unsigned*)(T_W + blk * 1024), 16, 0, 0);
  }
  v8bf mreg[8];
#pragma unroll
  for (int f = 0; f < 8; ++f) mreg[f] = *(const v8bf*)(ws + WS_WF + f * 1024 + lane * 16);
  float wc[32];
  {
    float s = 0.f;
#pragma unroll
    for (int j = 0; j < 4; ++j) {
      const v4u w = *(const v4u*)(wr + 32 * j + 8 * (lane >> 4));
#pragma unroll
      for (int i = 0; i < 4; ++i) { wc[8 * j + 2 * i] = bflo(w[i]); wc[8 * j + 2 * i + 1] = bfhi(w[i]); s += wc[8 * j + 2 * i] + wc[8 * j + 2 * i + 1]; }
    }
    s = add32(add16(s)) * (1.f / 128.f);
#pragma unroll
    for (int i = 0; i < 32; ++i) wc[i] = (wc[i] - s) * 1.4426950408889634f;
  }
  asm volatile("s_waitcnt vmcnt(0)" ::: "memory");
  __syncthreads();
  STAMPG(3, 1);
  tf_tiles<3>(3 * wave, pos0, mreg, wc, ws, out, lane);
  STAMPG(3, 2);
  if (wave == 0 && nt == 25) tf_tiles<1>(24, pos0, mreg, wc, ws, out, lane);
  STAMPG(3, 3);
}

extern "C" void kernel_launch(void* const* d_in, const int* in_sizes, int n_in, void* d_out, int out_size, void* d_ws, size_t ws_size, hipStream_t stream) {
  (void)in_sizes; (void)n_in; (void)out_size; (void)ws_size;
  char* ws = (char*)d_ws;
  (void)hipFuncSetAttribute((const void*)k_prep, hipFuncAttributeMaxDynamicSharedMemorySize, K1_LDS);
  (void)hipFuncSetAttribute((const void*)k_lists, hipFuncAttributeMaxDynamicSharedMemorySize, K2_LDS);
  (void)hipFuncSetAttribute((const void*)k_hops, hipFuncAttributeMaxDynamicSharedMemorySize, K3_LDS);
  (void)hipFuncSetAttribute((const void*)k_tf, hipFuncAttributeMaxDynamicSharedMemorySize, K4_LDS);
  k_prep<<<G1 + NODEWG + 2, 512, K1_LDS, stream>>>((const u16*)d_in[0], (const int*)d_in[1], (const int*)d_in[2], (const u16*)d_in[3],
                                                   (const u16*)d_in[4], (const u16*)d_in[5], (const u16*)d_in[6], (const u16*)d_in[7], ws);
  k_lists<<<NCH, 1024, K2_LDS, stream>>>(ws);
  k_hops<<<256, 1024, K3_LDS, stream>>>(ws);
  k_tf<<<256, 512, K4_LDS, stream>>>(ws, (const u16*)d_in[8], (u16*)d_out);
}
```

```cpp
#include <hip/hip_runtime.h>
#include <stdint.h>

typedef unsigned short u16;
typedef unsigned char u8;
typedef unsigned long long u64;
typedef __bf16 v8bf __attribute__((ext_vector_type(8)));
typedef __bf16 v4bf __attribute__((ext_vector_type(4)));
typedef __bf16 v2bf __attribute__((ext_vector_type(2)));
typedef float v4f __attribute__((ext_vector_type(4)));
typedef float v2f __attribute__((ext_vector_type(2)));
typedef float v16f __attribute__((ext_vector_type(16)));
typedef unsigned v4u __attribute__((ext_vector_type(4)));
typedef unsigned v2u __attribute__((ext_vector_type(2)));
typedef int v4i __attribute__((ext_vector_type(4)));
typedef short v4s __attribute__((ext_vector_type(4)));
typedef u16 v2h __attribute__((ext_vector_type(2)));
#define LAS __attribute__((address_space(3)))
#define GAS __attribute__((address_space(1)))
#if defined(__HIP_DEVICE_COMPILE__)
#define NOPK __attribute__((target("no-packed-fp32-ops")))
#else
#define NOPK
#endif
#define DEVI __device__ __forceinline__ NOPK
#define TID ((int)__builtin_amdgcn_workitem_id_x())
#define BID ((int)__builtin_amdgcn_workgroup_id_x())
DEVI float u2f(unsigned u) { return __builtin_bit_cast(float, u); }
DEVI unsigned f2u(float f) { return __builtin_bit_cast(unsigned, f); }
DEVI unsigned umin(unsigned a, unsigned b) { return a < b ? a : b; }
DEVI unsigned umax(unsigned a, unsigned b) { return a > b ? a : b; }
DEVI void wgsync() { __builtin_amdgcn_fence(__ATOMIC_RELEASE, "workgroup"); __builtin_amdgcn_s_barrier(); __builtin_amdgcn_fence(__ATOMIC_ACQUIRE, "workgroup"); }

constexpr int NN = 12500, NE = 400000;
constexpr int NCH = 200;
constexpr int NPOS = NCH * 64;
constexpr int SLB = NPOS * 8;
constexpr int HOPB = 32 * SLB;
constexpr int G1 = 248, CAP = 32, EPW = 807, NODEWG = 196;
constexpr int SEGB = G1 * CAP * 4;
constexpr size_t WS_B = 0;
constexpr size_t WS_SEG = WS_B + (size_t)9 * HOPB;
constexpr size_t WS_LIST = WS_SEG + (size_t)NCH * SEGB;
constexpr size_t WS_WF = WS_LIST + (size_t)NCH * 8192;
constexpr size_t WS_MRAW = WS_WF + 65536 + 8192 + 256;
constexpr size_t WS_RS = WS_MRAW + 65536;
constexpr size_t WS_INV = WS_RS + 12288;
constexpr size_t WS_PDIS = WS_INV + 25600;
constexpr size_t WS_PCI = WS_PDIS + 51200;
constexpr size_t WS_META = WS_PCI + 51200;
constexpr size_t WS_O1C = WS_META + 4096;
constexpr size_t WS_O1 = WS_O1C + 1024;
constexpr size_t WS_O2 = WS_O1 + (size_t)G1 * 2048 * 4;
constexpr size_t WS_DBG = WS_O2 + (size_t)NCH * SEGB;

#define SPINCAP (1u << 22)
#define STAMPV(kern, i, v) do {} while (0)
#define STAMPG(kern, i) do {} while (0)

DEVI LAS char* ldsbase(int a) { asm volatile("" : "+v"(a)); return (LAS char*)a; }
DEVI float bflo(unsigned u) { return u2f(u << 16); }
DEVI float bfhi(unsigned u) { return u2f(u & 0xffff0000u); }
DEVI unsigned pkbf(float a, float b) { v2f v = {a, b}; return __builtin_bit_cast(unsigned, __builtin_convertvector(v, v2bf)); }
DEVI __amdgpu_buffer_rsrc_t mkrsrc(const void* p, unsigned bytes) { return __builtin_amdgcn_make_buffer_rsrc((void*)p, 0, bytes, 0x00020000); }
#define store16(v, rs, voff, soff, aux) __builtin_amdgcn_raw_buffer_store_b128(v, rs, (voff) + (soff), 0, aux)
template <int C> DEVI unsigned dppu(unsigned x) { return (unsigned)__builtin_amdgcn_update_dpp(0, (int)x, C, 0xF, 0xF, true); }
template <int C> DEVI float dppf(float x) { return u2f(dppu<C>(f2u(x))); }
constexpr int DPP_X1 = 0xB1, DPP_X2 = 0x4E, DPP_HM = 0x141, DPP_RM = 0x140;
DEVI float add16(float x) { v2u r = __builtin_amdgcn_permlane16_swap(f2u(x), f2u(x), false, false); return u2f(r[0]) + u2f(r[1]); }
DEVI float add32(float x) { v2u r = __builtin_amdgcn_permlane32_swap(f2u(x), f2u(x), false, false); return u2f(r[0]) + u2f(r[1]); }
DEVI unsigned or16(unsigned x) { v2u r = __builtin_amdgcn_permlane16_swap(x, x, false, false); return r[0] | r[1]; }

DEVI bool anyl(bool c) { return __builtin_amdgcn_ballot_w64(c) != 0; }
constexpr unsigned LGRAN = 4;
template <int I> struct IC { static constexpr int v = I; };
DEVI unsigned key2(unsigned d, unsigned idx2) {
  v2h v = __builtin_bit_cast(v2h, d);
  v2h t = __builtin_elementwise_sub_sat((v2h){0x3F80, 0x3F80}, v);
  t = __builtin_elementwise_min(t, (v2h){4095, 4095});
  return (__builtin_bit_cast(unsigned, t) << 3) | idx2;
}
DEVI v4u keys8(v4u d) { return (v4u){key2(d[0], 0x00010000u), key2(d[1], 0x00030002u), key2(d[2], 0x00050004u), key2(d[3], 0x00070006u)}; }
DEVI unsigned rank8(v4u k, unsigned own) {
  const v2h o = __builtin_bit_cast(v2h, own | (own << 16));
  unsigned t[4];
#pragma unroll
  for (int i = 0; i < 4; ++i) { const unsigned ki = k[i]; const v2h df = __builtin_bit_cast(v2h, ki) - o; t[i] = __builtin_bit_cast(unsigned, df); }
  const unsigned a = __builtin_amdgcn_perm(t[0], t[1], 0x07050301u) & 0x80808080u;
  const unsigned b = __builtin_amdgcn_perm(t[2], t[3], 0x07050301u) & 0x80808080u;
  return __builtin_popcount(a) + __builtin_popcount(b);
}
DEVI unsigned chunk_of(unsigned w, unsigned r) {
  const u64 t0 = 0x150D0E09100C0800ull, t1 = 0x1611120A14020504ull, t2 = 0x17130F0B06070301ull;
  const unsigned j = w >> 3;
  const u64 t = j == 0 ? t0 : j == 1 ? t1 : t2;
  const unsigned slot = j == 3 ? 24u : (unsigned)(t >> (8 * r)) & 31u;
  return ((w + r) & 7) * 25 + slot;
}

#define K1AUX 0
constexpr int L1_LUT = 0, L1_CNT = 512, L1_ROW = L1_CNT + 1024, L1_D = L1_ROW + 64, L1_KEY = 2048, L1_SEG = L1_KEY + 25088, L1_X = L1_SEG + NCH * CAP * 4, K1_LDS = L1_X + 64 * 272;
DEVI unsigned pos_of(unsigned n) {
  const unsigned r = rank8(*(LAS v4u*)(L1_KEY + (n >> 3) * 16), *(LAS u16*)(L1_KEY + n * 2));
  return *(LAS u16*)(L1_LUT + ((n >> 9) * 8 + r) * 2) | ((n >> 3) & 63);
}
#define LDS_ADD(off, v) __hip_atomic_fetch_add((LAS unsigned*)(off), v, __ATOMIC_RELAXED, __HIP_MEMORY_SCOPE_WORKGROUP)
#define LDS_OR(off, v) (void)__hip_atomic_fetch_or((LAS unsigned*)(off), v, __ATOMIC_RELAXED, __HIP_MEMORY_SCOPE_WORKGROUP)
__global__ void __launch_bounds__(1024, 4) k_prep(const u16* __restrict__ x, const int* __restrict__ src, const int* __restrict__ dst, const u16* __restrict__ dis,
                                                  const u16* __restrict__ wq, const u16* __restrict__ wk, const u16* __restrict__ wv, const u16* __restrict__ wo, const u16* __restrict__ wr, char* __restrict__ ws) {
  extern __shared__ __attribute__((aligned(16))) char lds[];
  const int tid = TID, b = BID;
  STAMPG(0, 0);
  if (b < G1) {
    const __amdgpu_buffer_rsrc_t rdis = mkrsrc(dis, NN * 2), rx = mkrsrc(x, NN * 256), rb = mkrsrc(ws + WS_B, 9 * HOPB), rsg = mkrsrc(ws + WS_SEG, NCH * SEGB);
    const int q = b * EPW + tid, n0 = b * 64, t16 = tid * 16;
    const bool act = tid < EPW && q < NE / 2;
    v2u s2 = {0, 0}, d2 = {0, 0};
    if (act) { d2 = ((const v2u*)dst)[q]; s2 = ((const v2u*)src)[q]; }
    const v4u kd0 = __builtin_amdgcn_raw_buffer_load_b128(rdis, t16, 0, 0), kd1 = __builtin_amdgcn_raw_buffer_load_b128(rdis, t16, 16384, 0);
    const v4u xv = __builtin_amdgcn_raw_buffer_load_b128(rx, t16, n0 * 256, 0);
    const unsigned dn = __builtin_amdgcn_raw_buffer_load_b32(rdis, (tid & 62) * 2, n0 * 2, 0);
    {
      const v4u s = {~0u, ~0u, ~0u, ~0u};
      if (b < HOPB / 16384) store16(s, rb, t16, HOPB + b * 16384, 16);
      *(LAS v4u*)(L1_SEG + t16) = s;
      if (tid < NCH * CAP / 4 - 1024) *(LAS v4u*)(L1_SEG + 16384 + t16) = s;
    }
    if (tid < 256) { *(LAS unsigned*)(L1_CNT + tid * 4) = 0; *(LAS u16*)(L1_LUT + tid * 2) = (u16)(chunk_of(tid >> 3, tid & 7) * 64); }
    *(LAS v4u*)(L1_KEY + t16) = keys8(kd0);
    if (tid < 1568 - 1024) *(LAS v4u*)(L1_KEY + 16384 + t16) = keys8(kd1);
    __syncthreads();
    STAMPG(0, 1);
    if (act) {
      unsigned pd[2], ps[2], sl[2];
#pragma unroll
      for (int i = 0; i < 2; ++i) { pd[i] = pos_of(d2[i]); sl[i] = LDS_ADD(L1_CNT + (pd[i] >> 6) * 4, 1u); }
#pragma unroll
      for (int i = 0; i < 2; ++i) ps[i] = pos_of(s2[i]);
#pragma unroll
      for (int i = 0; i < 2; ++i) *(LAS unsigned*)(sl[i] < CAP ? L1_SEG + (pd[i] >> 6) * (CAP * 4) + sl[i] * 4 : L1_CNT + 1016) = ((pd[i] & 63) << 16) | ps[i];
      if (umax(sl[0], sl[1]) >= CAP) {
#pragma unroll
        for (int i = 0; i < 2; ++i)
          if (sl[i] >= CAP) ((unsigned*)(ws + WS_O1))[b * 2048 + LDS_ADD(L1_CNT + 1020, 1u)] = (pd[i] << 14) | ps[i];
      }
    }
    if (b < NODEWG) {
      *(LAS v4u*)(L1_X + (tid >> 4) * 272 + (tid & 15) * 16) = xv;
      if (tid >= 960) {
        const int i = tid & 63, n = n0 + i;
        const unsigned r = rank8(*(LAS v4u*)(L1_KEY + (n >> 3) * 16), *(LAS u16*)(L1_KEY + n * 2));
        const unsigned pos = chunk_of(n0 >> 9, r) * 64 + ((n >> 3) & 63);
        const float dv = (i & 1) ? bfhi(dn) : bflo(dn);
        *(LAS u8*)(L1_ROW + r * 8 + (i >> 3)) = (u8)i;
        *(LAS float*)(L1_D + (r * 8 + (i >> 3)) * 4) = dv;
        ((u16*)(ws + WS_INV))[pos] = n < NN ? (u16)n : (u16)0xFFFF;
        ((float*)(ws + WS_PDIS))[pos] = dv;
        ((float*)(ws + WS_PCI))[pos] = dv == 0.f ? 1.f : 1.f / dv;
      }
    }
    __syncthreads();
    STAMPG(0, 2);
    {
      const int so = (tid >> 3) * SEGB + (tid & 7) * 16 + b * (CAP * 4);
      store16(*(LAS v4u*)(L1_SEG + t16), rsg, so, 0, K1AUX);
      if (tid < NCH * CAP / 4 - 1024) store16(*(LAS v4u*)(L1_SEG + 16384 + t16), rsg, so, 128 * SEGB, K1AUX);
    }
    if (tid == 0) ((unsigned*)(ws + WS_O1C))[b] = *(LAS unsigned*)(L1_CNT + 1020);
    if (b < NODEWG) {
      const int u = tid & 3, s = (tid >> 2) & 31, r = __builtin_amdgcn_readfirstlane(tid >> 7);
      const unsigned rr = *(LAS u16*)(L1_ROW + r * 8 + 2 * u);
      const v2f dd = *(LAS v2f*)(L1_D + r * 32 + u * 8);
      const v2u a = *(LAS v2u*)(L1_X + (rr & 0xff) * 272 + s * 8), c = *(LAS v2u*)(L1_X + (rr >> 8) * 272 + s * 8);
      const unsigned off = s * SLB + u * 16 + (chunk_of(n0 >> 9, r) * 64 + ((n0 >> 3) & 63)) * 8;
      store16(((v4u){a[0], a[1], c[0], c[1]}), rb, off, 0, K1AUX);
      store16(((v4u){pkbf(bflo(a[0]) * dd[0], bfhi(a[0]) * dd[0]), pkbf(bflo(a[1]) * dd[0], bfhi(a[1]) * dd[0]), pkbf(bflo(c[0]) * dd[1], bfhi(c[0]) * dd[1]), pkbf(bflo(c[1]) * dd[1], bfhi(c[1]) * dd[1])}),
              rb, off, 8 * HOPB, K1AUX);
    }
    STAMPG(0, 4);
  } else {
    const int v = b - G1, which = __builtin_amdgcn_readfirstlane(tid >> 9), rt = (tid >> 6) & 7, lane = tid & 63, g = lane >> 4, c = lane & 15;
    const int e = 16 * v + (which == 0 ? (c & 3) | ((c & 4) << 1) | ((c & 8) >> 1) : c);
    const u16* wa = which == 0 ? wq : wv;
    v8bf af[4], bf[4];
#pragma unroll
    for (int ks = 0; ks < 4; ++ks) af[ks] = *(const v8bf*)(wa + (16 * rt + c) * 128 + 32 * ks + 8 * g);
    const float we = bflo(wr[e]);
    if (which == 0) {
#pragma unroll
      for (int ks = 0; ks < 4; ++ks) bf[ks] = *(const v8bf*)(wk + e * 128 + 32 * ks + 8 * g);
    } else {
      unsigned t[32];
#pragma unroll
      for (int i = 0; i < 32; ++i) t[i] = wo[(32 * (i >> 3) + 8 * g + (i & 7)) * 128 + e];
#pragma unroll
      for (int ks = 0; ks < 4; ++ks) bf[ks] = __builtin_bit_cast(v8bf, (v4u){t[8 * ks] | (t[8 * ks + 1] << 16), t[8 * ks + 2] | (t[8 * ks + 3] << 16), t[8 * ks + 4] | (t[8 * ks + 5] << 16), t[8 * ks + 6] | (t[8 * ks + 7] << 16)});
    }
    v4f acc = {0, 0, 0, 0};
#pragma unroll
    for (int ks = 0; ks < 4; ++ks) acc = __builtin_amdgcn_mfma_f32_16x16x32_bf16(af[ks], bf[ks], acc, 0, 0, 0);
    const float cs = add32(add16((acc[0] + acc[1]) + (acc[2] + acc[3])));
    float* csL = (float*)lds;
    if (lane < 16) csL[(which * 16 + c) * 8 + rt] = cs;
    __syncthreads();
    const v4f p0 = *(const v4f*)(csL + (which * 16 + c) * 8), p1 = *(const v4f*)(csL + (which * 16 + c) * 8 + 4);
    acc -= (((p0[0] + p0[1]) + (p0[2] + p0[3])) + ((p1[0] + p1[1]) + (p1[2] + p1[3]))) * (1.f / 128.f);
    if (which == 0) *(v4f*)(ws + WS_MRAW + ((v * 8 + rt) * 64 + lane) * 16) = acc;
    else *(v2u*)(ws + WS_WF + 32768 + (((v >> 1) * 8 + rt) * 64 + 32 * (g >> 1) + 16 * (v & 1) + c) * 16 + 8 * (g & 1)) = (v2u){pkbf(acc[0], acc[1]), pkbf(acc[2], acc[3])};
    v4f t;
#pragma unroll
    for (int r = 0; r < 4; ++r) {
      const float u = acc[r] * we;
      float x0 = (c & 1) ? u : acc[r];
      x0 += dppf<DPP_X1>((c & 1) ? acc[r] : u); x0 += dppf<DPP_X2>(x0); x0 += dppf<0x124>(x0); x0 += dppf<0x128>(x0);
      t[r] = x0;
    }
    if (c <= which) *(v4f*)(ws + WS_RS + (((which + c) * 8 + v) * 128 + 16 * rt + 4 * g) * 4) = t;
    STAMPG(0, 9);
  }
}

constexpr int L2_CNT = 0, L2_MSK = 8192, L2_OUT = L2_MSK + 2048 + 64, L2_PICK = L2_OUT + 8192, L2_EXT = L2_PICK + 16384, K2_LDS = L2_EXT + G1 * CAP * 4;
constexpr int M_E2 = L2_MSK + 512, M_BK = L2_MSK + 768, M_DG = L2_MSK + 1024, M_LO = L2_MSK + 1280, M_FR = L2_MSK + 1536, M_OV = L2_MSK + 2048;
DEVI int kth_high(v2u m, unsigned k) {
  const unsigned ch = __builtin_popcount(m[1]);
  const bool hi = k < ch;
  unsigned w = hi ? m[1] : m[0];
  k = hi ? k : k - ch;
  if (k >= (unsigned)__builtin_popcount(w)) return -1;
  int pos = hi ? 32 : 0;
#pragma unroll
  for (int sh = 16; sh >= 1; sh >>= 1) {
    const unsigned up = w >> sh, cu = __builtin_popcount(up);
    const bool t = k < cu;
    w = t ? up : w & ((1u << sh) - 1u);
    k = t ? k : k - cu;
    pos += t ? sh : 0;
  }
  return pos;
}
__global__ void __launch_bounds__(1024, 4) k_lists(char* __restrict__ ws, const u16* __restrict__ wr) {
  extern __shared__ __attribute__((aligned(16))) char lds[];
  const int tid = TID, ch = BID, lane = tid & 63, wave = __builtin_amdgcn_readfirstlane(tid >> 6);
  STAMPG(1, 0);
  if (ch >= NCH) {
    const float* rs = (const float*)(ws + WS_RS);
    if (ch < NCH + 4) {
      const int it = (ch - NCH) * 16 + wave, v = it >> 3, rt = it & 7, g = lane >> 4, c = lane & 15;
      v4f a = *(const v4f*)(ws + WS_MRAW + (it * 64 + lane) * 16), m = {0, 0, 0, 0};
#pragma unroll
      for (int w = 0; w < 8; ++w) m += *(const v4f*)(rs + w * 128 + 16 * rt + 4 * g);
      a = (a - m * (1.f / 128.f)) * (1.4426950408889634f * 0.08838834764831845f);
      *(v2u*)(ws + WS_WF + (((v >> 1) * 8 + rt) * 64 + 32 * (g >> 1) + 16 * (v & 1) + c) * 16 + 8 * (g & 1)) = (v2u){pkbf(a[0], a[1]), pkbf(a[2], a[3])};
    } else {
      const int g = lane >> 4, c = lane & 15, d0 = 16 * (wave >> 1) + 8 * (g >> 1) + 4 * (wave & 1);
      v4f m = {0, 0, 0, 0};
#pragma unroll
      for (int w = 0; w < 8; ++w) m += *(const v4f*)(rs + ((1 + (c & 1)) * 8 + w) * 128 + d0);
      const v2u w2 = *(const v2u*)(wr + d0);
      v2u o = (c & 2) ? (v2u){pkbf(m[0], m[1]), pkbf(m[2], m[3])} : (c & 1) ? w2 : (v2u){0x3F803F80u, 0x3F803F80u};
      if (((c >> 2) ^ g) & 1) o = (v2u){0u, 0u};
      *(v2u*)(ws + WS_WF + 65536 + ((wave >> 1) * 64 + lane) * 16 + 8 * (wave & 1)) = o;
      if (wave == 0) {
        const unsigned wv = ((const unsigned*)wr)[lane];
        float s = bflo(wv) + bfhi(wv);
        s += dppf<DPP_X1>(s); s += dppf<DPP_X2>(s); s += dppf<DPP_HM>(s); s += dppf<DPP_RM>(s);
        s = add32(add16(s));
        if (lane == 0) *(float*)(ws + WS_WF + 65536 + 8192) = s * (1.f / 128.f);
      }
    }
    return;
  }
  unsigned* o2 = (unsigned*)(ws + WS_O2 + (size_t)ch * SEGB);
  v4u e4[2] = {{~0u, ~0u, ~0u, ~0u}, {~0u, ~0u, ~0u, ~0u}};
  if (tid < G1 * 4) {
    const v4u* seg = (const v4u*)(ws + WS_SEG + (size_t)ch * SEGB) + (tid >> 2) * 8 + (tid & 3);
    e4[0] = seg[0]; e4[1] = seg[4];
  }
  const unsigned o1c = tid < G1 ? ((const unsigned*)(ws + WS_O1C))[tid] : 0u;
  if (tid < L2_OUT / 16) *(LAS v4u*)(tid * 16) = (v4u){0, 0, 0, 0};
  if (tid >= 512) *(LAS v4u*)(L2_OUT - 8192 + tid * 16) = (v4u){~0u, ~0u, ~0u, ~0u};
  __syncthreads();
  STAMPG(1, 1);
  const bool more = anyl(e4[1][0] != ~0u);
  auto each = [&](auto&& f) __attribute__((always_inline)) {
#pragma unroll
    for (int i = 0; i < 4; ++i) { unsigned e = e4[0][i]; f(e); e4[0][i] = e; }
    if (more) {
#pragma unroll
      for (int i = 0; i < 4; ++i) { unsigned e = e4[1][i]; f(e); e4[1][i] = e; }
    }
  };
  each([&](unsigned& e) __attribute__((always_inline)) {
    const unsigned ln = e >> 16;
    e |= umin(LDS_ADD(L2_CNT + ((e ^ ln) & 31) * 256 + ln * 4, 1u), 3u) << 22;
  });
  if (o1c) *(LAS unsigned*)(M_OV + 4) = 1;
  __syncthreads();
  STAMPG(1, 2);
  auto place4 = [&](const v4u ev) __attribute__((always_inline)) {
    unsigned off[4], tot = 0;
#pragma unroll
    for (int i = 0; i < 4; ++i) {
      const u64 m = __builtin_amdgcn_ballot_w64((ev[i] >> 16) - 64u < 192u);
      off[i] = __builtin_amdgcn_mbcnt_hi((unsigned)(m >> 32), __builtin_amdgcn_mbcnt_lo((unsigned)m, tot));
      tot += __builtin_popcountll(m);
    }
    unsigned base;
    asm volatile("ds_add_rtn_u32 %0, %1, %2\n s_waitcnt lgkmcnt(0)" : "=&v"(base) : "v"(lane == 0 ? M_OV + 12 : 0x7FFF0), "v"(tot) : "memory");
    base = __builtin_amdgcn_readfirstlane(base) * 4 + L2_EXT;
#pragma unroll
    for (int i = 0; i < 4; ++i) {
      const unsigned e = ev[i], ln = e >> 16, d = (e ^ ln) & 31;
      *(LAS u16*)(ln < 64 ? L2_OUT + (d >> 3) * 1024 + ln * 16 + (d & 7) * 2 : 0x7FFF0) = (u16)e;
      *(LAS unsigned*)(ln - 64u < 192u ? base + off[i] * 4 : 0x7FFF0) = e;
    }
  };
  auto place = [&]() __attribute__((always_inline)) { place4(e4[0]); if (more) place4(e4[1]); };
  if (wave < 2) place();
  else if (wave >= 8) {
    const int d = 4 * (wave - 8);
    unsigned u0 = 0, u1 = 0, u2 = 0, dg = 0;
#pragma unroll
    for (int i = 0; i < 4; ++i) {
      const unsigned n = *(LAS unsigned*)(L2_CNT + (d + i) * 256 + lane * 4);
      dg += n;
      u0 |= umin(n, 1u) << i; u1 |= (n > 1 ? 1u : 0u) << i; u2 |= (n > 2 ? 1u : 0u) << i;
    }
    (void)__hip_atomic_fetch_or((LAS u64*)(L2_MSK + lane * 8), (u64)(u0 << d) | ((u64)(u1 << d) << 32), __ATOMIC_RELAXED, __HIP_MEMORY_SCOPE_WORKGROUP);
    LDS_OR(M_E2 + lane * 4, u2 << d);
    LDS_OR(M_BK + (lane ^ d) * 4, ((u0 & 1u) | (dppu<DPP_X1>(u0) & 2u) | (dppu<DPP_X2>(u0) & 4u) | (dppu<0x1B>(u0) & 8u)) << d);
    (void)LDS_ADD(M_DG + lane * 4, dg);
  }
  __syncthreads();
  STAMPG(1, 3);
  if (wave < 2) {
    __builtin_amdgcn_s_setprio(3);
    unsigned lf, bf, ex;
    if (wave == 0) { const v2u t = *(LAS v2u*)(L2_MSK + lane * 8); lf = ~t[0]; ex = t[1]; bf = ~*(LAS unsigned*)(M_BK + lane * 4); }
    else {
      unsigned dg = *(LAS unsigned*)(M_DG + lane * 4);
      ex = *(LAS unsigned*)(M_E2 + lane * 4);
      dg = umax(dg, dppu<DPP_X1>(dg)); dg = umax(dg, dppu<DPP_X2>(dg)); dg = umax(dg, dppu<DPP_HM>(dg)); dg = umax(dg, dppu<DPP_RM>(dg));
      const unsigned m = umax(umax((unsigned)__builtin_amdgcn_readlane(dg, 0), (unsigned)__builtin_amdgcn_readlane(dg, 16)), umax((unsigned)__builtin_amdgcn_readlane(dg, 32), (unsigned)__builtin_amdgcn_readlane(dg, 48)));
      const unsigned L = umin(umax((m + LGRAN - 1) & ~(LGRAN - 1u), 32u), 64u);
      if (lane == 0) *(LAS unsigned*)(M_OV + 8) = L;
      lf = bf = (unsigned)((1ull << (L - 32)) - 1ull);
    }
    LAS unsigned* pk = (LAS unsigned*)(L2_PICK + wave * 8192 + lane * 4);
    int d = 0;
#pragma unroll
    for (int i = 0; i < 32; ++i) {
      if (i) {
        const int t = __builtin_ctz(i);
        if (t == 0) { bf = dppu<DPP_X1>(bf); d ^= 1; }
        else if (t == 1) { bf = dppu<DPP_X2>(bf); d ^= 2; }
        else if (t == 2) { bf = dppu<DPP_HM>(bf); d ^= 7; }
        else if (t == 3) { bf = dppu<DPP_RM>(bf); d ^= 15; }
        else { const v2u r = __builtin_amdgcn_permlane16_swap(bf, bf, false, false); bf = (lane & 16) ? r[0] : r[1]; d ^= 16; }
      }
      const unsigned f0 = lf & bf;
      const unsigned p = f0 & ~(f0 - ((ex >> d) & 1u));
      lf ^= p; bf ^= p;
      pk[d * 64] = p;
    }
    *(LAS unsigned*)(M_FR + lane * 8 + wave * 4) = lf;
  } else place();
  __syncthreads();
  STAMPG(1, 4);
  const unsigned nex = *(LAS unsigned*)(M_OV + 12);
  for (unsigned i = tid; i < nex; i += 1024) {
    const unsigned e = *(LAS unsigned*)(L2_EXT + i * 4), rk = e >> 22, ln = (e >> 16) & 63, d = (e ^ ln) & 31;
    const unsigned m = rk < 3 ? *(LAS unsigned*)(L2_PICK - 8192 + rk * 8192 + d * 256 + ln * 4) : 0u;
    int c;
    if (m) c = __builtin_ctz(m) + 32 * rk - 32;
    else c = kth_high(*(LAS v2u*)(M_FR + ln * 8), LDS_ADD(M_LO + ln * 4, 1u));
    if (c >= 0) *(LAS u16*)(L2_OUT + (c >> 3) * 1024 + ln * 16 + (c & 7) * 2) = (u16)e;
    else o2[LDS_ADD(M_OV, 1u)] = e & 0x3FFFFFu;
  }
  __syncthreads();
  STAMPG(1, 5);
  const unsigned L = *(LAS unsigned*)(M_OV + 8);
  if (8 * wave < L) *(v4u*)(ws + WS_LIST + (size_t)ch * 8192 + tid * 16) = *(LAS v4u*)(L2_OUT + tid * 16);
  if (tid == 1023) *(v4u*)(ws + WS_META + ch * 16) = (v4u){L >> 1, *(LAS unsigned*)(M_OV), *(LAS unsigned*)(M_OV + 4), 0u};
  STAMPG(1, 6);
}

#define GATHER_ASM \
  "s_waitcnt lgkmcnt(0)\n" \
  "v_lshlrev_b32_sdwa v8, 3, %4 dst_sel:DWORD dst_unused:UNUSED_PAD src0_sel:DWORD src1_sel:WORD_0\n" \
  "v_lshlrev_b32_sdwa v10, 3, %4 dst_sel:DWORD dst_unused:UNUSED_PAD src0_sel:DWORD src1_sel:WORD_1\n" \
  "ds_read_b64 v[8:9], v8\n" \
  "ds_read_b64 v[10:11], v10\n" \
  "v_lshlrev_b32_sdwa v12, 3, %5 dst_sel:DWORD dst_unused:UNUSED_PAD src0_sel:DWORD src1_sel:WORD_0\n" \
  "v_lshlrev_b32_sdwa v14, 3, %5 dst_sel:DWORD dst_unused:UNUSED_PAD src0_sel:DWORD src1_sel:WORD_1\n" \
  "ds_read_b64 v[12:13], v12\n" \
  "ds_read_b64 v[14:15], v14\n" \
  "v_lshlrev_b32_sdwa v16, 3, %6 dst_sel:DWORD dst_unused:UNUSED_PAD src0_sel:DWORD src1_sel:WORD_0\n" \
  "v_lshlrev_b32_sdwa v18, 3, %6 dst_sel:DWORD dst_unused:UNUSED_PAD src0_sel:DWORD src1_sel:WORD_1\n" \
  "ds_read_b64 v[16:17], v16\n" \
  "ds_read_b64 v[18:19], v18\n" \
  "v_lshlrev_b32_sdwa v20, 3, %7 dst_sel:DWORD dst_unused:UNUSED_PAD src0_sel:DWORD src1_sel:WORD_0\n" \
  "v_lshlrev_b32_sdwa v22, 3, %7 dst_sel:DWORD dst_unused:UNUSED_PAD src0_sel:DWORD src1_sel:WORD_1\n" \
  "ds_read_b64 v[20:21], v20\n" \
  "ds_read_b64 v[22:23], v22\n" \
  "s_waitcnt lgkmcnt(4)\n" \
  "v_smfmac_f32_16x16x64_bf16 %0, %1, v[8:15], %2\n" \
  "v_lshlrev_b32_sdwa v8, 3, %8 dst_sel:DWORD dst_unused:UNUSED_PAD src0_sel:DWORD src1_sel:WORD_0\n" \
  "v_lshlrev_b32_sdwa v10, 3, %8 dst_sel:DWORD dst_unused:UNUSED_PAD src0_sel:DWORD src1_sel:WORD_1\n" \
  "ds_read_b64 v[8:9], v8\n" \
  "ds_read_b64 v[10:11], v10\n" \
  "v_lshlrev_b32_sdwa v12, 3, %9 dst_sel:DWORD dst_unused:UNUSED_PAD src0_sel:DWORD src1_sel:WORD_0\n" \
  "v_lshlrev_b32_sdwa v14, 3, %9 dst_sel:DWORD dst_unused:UNUSED_PAD src0_sel:DWORD src1_sel:WORD_1\n" \
  "ds_read_b64 v[12:13], v12\n" \
  "ds_read_b64 v[14:15], v14\n" \
  "s_waitcnt lgkmcnt(4)\n" \
  "v_smfmac_f32_16x16x64_bf16 %0, %1, v[16:23], %2\n" \
  "v_lshlrev_b32_sdwa v16, 3, %10 dst_sel:DWORD dst_unused:UNUSED_PAD src0_sel:DWORD src1_sel:WORD_0\n" \
  "v_lshlrev_b32_sdwa v18, 3, %10 dst_sel:DWORD dst_unused:UNUSED_PAD src0_sel:DWORD src1_sel:WORD_1\n" \
  "ds_read_b64 v[16:17], v16\n" \
  "ds_read_b64 v[18:19], v18\n" \
  "v_lshlrev_b32_sdwa v20, 3, %11 dst_sel:DWORD dst_unused:UNUSED_PAD src0_sel:DWORD src1_sel:WORD_0\n" \
  "v_lshlrev_b32_sdwa v22, 3, %11 dst_sel:DWORD dst_unused:UNUSED_PAD src0_sel:DWORD src1_sel:WORD_1\n" \
  "ds_read_b64 v[20:21], v20\n" \
  "ds_read_b64 v[22:23], v22\n" \
  "s_waitcnt lgkmcnt(4)\n" \
  "v_smfmac_f32_16x16x64_bf16 %0, %1, v[8:15], %2\n" \
  "v_lshlrev_b32_sdwa v8, 3, %12 dst_sel:DWORD dst_unused:UNUSED_PAD src0_sel:DWORD src1_sel:WORD_0\n" \
  "v_lshlrev_b32_sdwa v10, 3, %12 dst_sel:DWORD dst_unused:UNUSED_PAD src0_sel:DWORD src1_sel:WORD_1\n" \
  "ds_read_b64 v[8:9], v8\n" \
  "ds_read_b64 v[10:11], v10\n" \
  "v_lshlrev_b32_sdwa v12, 3, %13 dst_sel:DWORD dst_unused:UNUSED_PAD src0_sel:DWORD src1_sel:WORD_0\n" \
  "v_lshlrev_b32_sdwa v14, 3, %13 dst_sel:DWORD dst_unused:UNUSED_PAD src0_sel:DWORD src1_sel:WORD_1\n" \
  "ds_read_b64 v[12:13], v12\n" \
  "ds_read_b64 v[14:15], v14\n" \
  "s_waitcnt lgkmcnt(4)\n" \
  "v_smfmac_f32_16x16x64_bf16 %0, %1, v[16:23], %2\n" \
  "v_lshlrev_b32_sdwa v16, 3, %14 dst_sel:DWORD dst_unused:UNUSED_PAD src0_sel:DWORD src1_sel:WORD_0\n" \
  "v_lshlrev_b32_sdwa v18, 3, %14 dst_sel:DWORD dst_unused:UNUSED_PAD src0_sel:DWORD src1_sel:WORD_1\n" \
  "ds_read_b64 v[16:17], v16\n" \
  "ds_read_b64 v[18:19], v18\n" \
  "v_lshlrev_b32_sdwa v20, 3, %15 dst_sel:DWORD dst_unused:UNUSED_PAD src0_sel:DWORD src1_sel:WORD_0\n" \
  "v_lshlrev_b32_sdwa v22, 3, %15 dst_sel:DWORD dst_unused:UNUSED_PAD src0_sel:DWORD src1_sel:WORD_1\n" \
  "ds_read_b64 v[20:21], v20\n" \
  "ds_read_b64 v[22:23], v22\n" \
  "s_waitcnt lgkmcnt(4)\n" \
  "v_smfmac_f32_16x16x64_bf16 %0, %1, v[8:15], %2\n" \
  "v_lshlrev_b32_sdwa v8, 3, %16 dst_sel:DWORD dst_unused:UNUSED_PAD src0_sel:DWORD src1_sel:WORD_0\n" \
  "v_lshlrev_b32_sdwa v10, 3, %16 dst_sel:DWORD dst_unused:UNUSED_PAD src0_sel:DWORD src1_sel:WORD_1\n" \
  "ds_read_b64 v[8:9], v8\n" \
  "ds_read_b64 v[10:11], v10\n" \
  "v_lshlrev_b32_sdwa v12, 3, %17 dst_sel:DWORD dst_unused:UNUSED_PAD src0_sel:DWORD src1_sel:WORD_0\n" \
  "v_lshlrev_b32_sdwa v14, 3, %17 dst_sel:DWORD dst_unused:UNUSED_PAD src0_sel:DWORD src1_sel:WORD_1\n" \
  "ds_read_b64 v[12:13], v12\n" \
  "ds_read_b64 v[14:15], v14\n" \
  "s_waitcnt lgkmcnt(4)\n" \
  "v_smfmac_f32_16x16x64_bf16 %0, %1, v[16:23], %2\n" \
  "v_lshlrev_b32_sdwa v16, 3, %18 dst_sel:DWORD dst_unused:UNUSED_PAD src0_sel:DWORD src1_sel:WORD_0\n" \
  "v_lshlrev_b32_sdwa v18, 3, %18 dst_sel:DWORD dst_unused:UNUSED_PAD src0_sel:DWORD src1_sel:WORD_1\n" \
  "ds_read_b64 v[16:17], v16\n" \
  "ds_read_b64 v[18:19], v18\n" \
  "v_lshlrev_b32_sdwa v20, 3, %19 dst_sel:DWORD dst_unused:UNUSED_PAD src0_sel:DWORD src1_sel:WORD_0\n" \
  "v_lshlrev_b32_sdwa v22, 3, %19 dst_sel:DWORD dst_unused:UNUSED_PAD src0_sel:DWORD src1_sel:WORD_1\n" \
  "ds_read_b64 v[20:21], v20\n" \
  "ds_read_b64 v[22:23], v22\n" \
  "s_cmp_le_u32 %3, 16\n" \
  "s_cbranch_scc1 Lgt%=\n" \
  "s_waitcnt lgkmcnt(4)\n" \
  "v_smfmac_f32_16x16x64_bf16 %0, %1, v[8:15], %2\n" \
  "v_lshlrev_b32_sdwa v8, 3, %20 dst_sel:DWORD dst_unused:UNUSED_PAD src0_sel:DWORD src1_sel:WORD_0\n" \
  "v_lshlrev_b32_sdwa v10, 3, %20 dst_sel:DWORD dst_unused:UNUSED_PAD src0_sel:DWORD src1_sel:WORD_1\n" \
  "ds_read_b64 v[8:9], v8\n" \
  "ds_read_b64 v[10:11], v10\n" \
  "v_lshlrev_b32_sdwa v12, 3, %21 dst_sel:DWORD dst_unused:UNUSED_PAD src0_sel:DWORD src1_sel:WORD_0\n" \
  "v_lshlrev_b32_sdwa v14, 3, %21 dst_sel:DWORD dst_unused:UNUSED_PAD src0_sel:DWORD src1_sel:WORD_1\n" \
  "ds_read_b64 v[12:13], v12\n" \
  "ds_read_b64 v[14:15], v14\n" \
  "s_cmp_le_u32 %3, 18\n" \
  "s_cbranch_scc1 Lgt%=\n" \
  "s_waitcnt lgkmcnt(4)\n" \
  "v_smfmac_f32_16x16x64_bf16 %0, %1, v[16:23], %2\n" \
  "v_lshlrev_b32_sdwa v16, 3, %22 dst_sel:DWORD dst_unused:UNUSED_PAD src0_sel:DWORD src1_sel:WORD_0\n" \
  "v_lshlrev_b32_sdwa v18, 3, %22 dst_sel:DWORD dst_unused:UNUSED_PAD src0_sel:DWORD src1_sel:WORD_1\n" \
  "ds_read_b64 v[16:17], v16\n" \
  "ds_read_b64 v[18:19], v18\n" \
  "v_lshlrev_b32_sdwa v20, 3, %23 dst_sel:DWORD dst_unused:UNUSED_PAD src0_sel:DWORD src1_sel:WORD_0\n" \
  "v_lshlrev_b32_sdwa v22, 3, %23 dst_sel:DWORD dst_unused:UNUSED_PAD src0_sel:DWORD src1_sel:WORD_1\n" \
  "ds_read_b64 v[20:21], v20\n" \
  "ds_read_b64 v[22:23], v22\n" \
  "s_cmp_le_u32 %3, 20\n" \
  "s_cbranch_scc1 Lgt%=\n" \
  "s_waitcnt lgkmcnt(4)\n" \
  "v_smfmac_f32_16x16x64_bf16 %0, %1, v[8:15], %2\n" \
  "v_lshlrev_b32_sdwa v8, 3, %24 dst_sel:DWORD dst_unused:UNUSED_PAD src0_sel:DWORD src1_sel:WORD_0\n" \
  "v_lshlrev_b32_sdwa v10, 3, %24 dst_sel:DWORD dst_unused:UNUSED_PAD src0_sel:DWORD src1_sel:WORD_1\n" \
  "ds_read_b64 v[8:9], v8\n" \
  "ds_read_b64 v[10:11], v10\n" \
  "v_lshlrev_b32_sdwa v12, 3, %25 dst_sel:DWORD dst_unused:UNUSED_PAD src0_sel:DWORD src1_sel:WORD_0\n" \
  "v_lshlrev_b32_sdwa v14, 3, %25 dst_sel:DWORD dst_unused:UNUSED_PAD src0_sel:DWORD src1_sel:WORD_1\n" \
  "ds_read_b64 v[12:13], v12\n" \
  "ds_read_b64 v[14:15], v14\n" \
  "s_cmp_le_u32 %3, 22\n" \
  "s_cbranch_scc1 Lgt%=\n" \
  "s_waitcnt lgkmcnt(4)\n" \
  "v_smfmac_f32_16x16x64_bf16 %0, %1, v[16:23], %2\n" \
  "v_lshlrev_b32_sdwa v16, 3, %26 dst_sel:DWORD dst_unused:UNUSED_PAD src0_sel:DWORD src1_sel:WORD_0\n" \
  "v_lshlrev_b32_sdwa v18, 3, %26 dst_sel:DWORD dst_unused:UNUSED_PAD src0_sel:DWORD src1_sel:WORD_1\n" \
  "ds_read_b64 v[16:17], v16\n" \
  "ds_read_b64 v[18:19], v18\n" \
  "v_lshlrev_b32_sdwa v20, 3, %27 dst_sel:DWORD dst_unused:UNUSED_PAD src0_sel:DWORD src1_sel:WORD_0\n" \
  "v_lshlrev_b32_sdwa v22, 3, %27 dst_sel:DWORD dst_unused:UNUSED_PAD src0_sel:DWORD src1_sel:WORD_1\n" \
  "ds_read_b64 v[20:21], v20\n" \
  "ds_read_b64 v[22:23], v22\n" \
  "s_cmp_le_u32 %3, 24\n" \
  "s_cbranch_scc1 Lgt%=\n" \
  "s_waitcnt lgkmcnt(4)\n" \
  "v_smfmac_f32_16x16x64_bf16 %0, %1, v[8:15], %2\n" \
  "v_lshlrev_b32_sdwa v8, 3, %28 dst_sel:DWORD dst_unused:UNUSED_PAD src0_sel:DWORD src1_sel:WORD_0\n" \
  "v_lshlrev_b32_sdwa v10, 3, %28 dst_sel:DWORD dst_unused:UNUSED_PAD src0_sel:DWORD src1_sel:WORD_1\n" \
  "ds_read_b64 v[8:9], v8\n" \
  "ds_read_b64 v[10:11], v10\n" \
  "v_lshlrev_b32_sdwa v12, 3, %29 dst_sel:DWORD dst_unused:UNUSED_PAD src0_sel:DWORD src1_sel:WORD_0\n" \
  "v_lshlrev_b32_sdwa v14, 3, %29 dst_sel:DWORD dst_unused:UNUSED_PAD src0_sel:DWORD src1_sel:WORD_1\n" \
  "ds_read_b64 v[12:13], v12\n" \
  "ds_read_b64 v[14:15], v14\n" \
  "s_cmp_le_u32 %3, 26\n" \
  "s_cbranch_scc1 Lgt%=\n" \
  "s_waitcnt lgkmcnt(4)\n" \
  "v_smfmac_f32_16x16x64_bf16 %0, %1, v[16:23], %2\n" \
  "v_lshlrev_b32_sdwa v16, 3, %30 dst_sel:DWORD dst_unused:UNUSED_PAD src0_sel:DWORD src1_sel:WORD_0\n" \
  "v_lshlrev_b32_sdwa v18, 3, %30 dst_sel:DWORD dst_unused:UNUSED_PAD src0_sel:DWORD src1_sel:WORD_1\n" \
  "ds_read_b64 v[16:17], v16\n" \
  "ds_read_b64 v[18:19], v18\n" \
  "v_lshlrev_b32_sdwa v20, 3, %31 dst_sel:DWORD dst_unused:UNUSED_PAD src0_sel:DWORD src1_sel:WORD_0\n" \
  "v_lshlrev_b32_sdwa v22, 3, %31 dst_sel:DWORD dst_unused:UNUSED_PAD src0_sel:DWORD src1_sel:WORD_1\n" \
  "ds_read_b64 v[20:21], v20\n" \
  "ds_read_b64 v[22:23], v22\n" \
  "s_cmp_le_u32 %3, 28\n" \
  "s_cbranch_scc1 Lgt%=\n" \
  "s_waitcnt lgkmcnt(4)\n" \
  "v_smfmac_f32_16x16x64_bf16 %0, %1, v[8:15], %2\n" \
  "v_lshlrev_b32_sdwa v8, 3, %32 dst_sel:DWORD dst_unused:UNUSED_PAD src0_sel:DWORD src1_sel:WORD_0\n" \
  "v_lshlrev_b32_sdwa v10, 3, %32 dst_sel:DWORD dst_unused:UNUSED_PAD src0_sel:DWORD src1_sel:WORD_1\n" \
  "ds_read_b64 v[8:9], v8\n" \
  "ds_read_b64 v[10:11], v10\n" \
  "v_lshlrev_b32_sdwa v12, 3, %33 dst_sel:DWORD dst_unused:UNUSED_PAD src0_sel:DWORD src1_sel:WORD_0\n" \
  "v_lshlrev_b32_sdwa v14, 3, %33 dst_sel:DWORD dst_unused:UNUSED_PAD src0_sel:DWORD src1_sel:WORD_1\n" \
  "ds_read_b64 v[12:13], v12\n" \
  "ds_read_b64 v[14:15], v14\n" \
  "s_cmp_le_u32 %3, 30\n" \
  "s_cbranch_scc1 Lgt%=\n" \
  "s_waitcnt lgkmcnt(4)\n" \
  "v_smfmac_f32_16x16x64_bf16 %0, %1, v[16:23], %2\n" \
  "v_lshlrev_b32_sdwa v16, 3, %34 dst_sel:DWORD dst_unused:UNUSED_PAD src0_sel:DWORD src1_sel:WORD_0\n" \
  "v_lshlrev_b32_sdwa v18, 3, %34 dst_sel:DWORD dst_unused:UNUSED_PAD src0_sel:DWORD src1_sel:WORD_1\n" \
  "ds_read_b64 v[16:17], v16\n" \
  "ds_read_b64 v[18:19], v18\n" \
  "v_lshlrev_b32_sdwa v20, 3, %35 dst_sel:DWORD dst_unused:UNUSED_PAD src0_sel:DWORD src1_sel:WORD_0\n" \
  "v_lshlrev_b32_sdwa v22, 3, %35 dst_sel:DWORD dst_unused:UNUSED_PAD src0_sel:DWORD src1_sel:WORD_1\n" \
  "ds_read_b64 v[20:21], v20\n" \
  "ds_read_b64 v[22:23], v22\n" \
  "Lgt%=:\n" \
  "s_waitcnt lgkmcnt(0)\n" \
  "v_smfmac_f32_16x16x64_bf16 %0, %1, v[8:15], %2\n" \
  "v_smfmac_f32_16x16x64_bf16 %0, %1, v[16:23], %2\n" \
  "s_nop 7\n" \
  ""
#define GATHER_CLOB "v8", "v9", "v10", "v11", "v12", "v13", "v14", "v15", "v16", "v17", "v18", "v19", "v20", "v21", "v22", "v23", "scc"

DEVI v4f gather(const v4u (&L)[8], int n, v8bf sel, int sidx) {
  v4f acc = {0.f, 0.f, 0.f, 0.f};
  asm volatile(GATHER_ASM : "+v"(acc) : "v"(sel), "v"(sidx), "s"(n),
    "v"(L[0][0]), "v"(L[0][1]), "v"(L[0][2]), "v"(L[0][3]), "v"(L[1][0]), "v"(L[1][1]), "v"(L[1][2]), "v"(L[1][3]),
    "v"(L[2][0]), "v"(L[2][1]), "v"(L[2][2]), "v"(L[2][3]), "v"(L[3][0]), "v"(L[3][1]), "v"(L[3][2]), "v"(L[3][3]),
    "v"(L[4][0]), "v"(L[4][1]), "v"(L[4][2]), "v"(L[4][3]), "v"(L[5][0]), "v"(L[5][1]), "v"(L[5][2]), "v"(L[5][3]),
    "v"(L[6][0]), "v"(L[6][1]), "v"(L[6][2]), "v"(L[6][3]), "v"(L[7][0]), "v"(L[7][1]), "v"(L[7][2]), "v"(L[7][3])
    : GATHER_CLOB, "memory");
  return acc;
}
DEVI v4f slow_edges(v4f acc, const char* ws, int ch, int n2, int any1, int lane) {
  const unsigned* o2 = (const unsigned*)(ws + WS_O2 + (size_t)ch * SEGB);
  const int lists = any1 ? G1 : 0;
  for (int li = -1; li < lists; ++li) {
    const int cnt = li < 0 ? n2 : (int)((const unsigned*)(ws + WS_O1C))[li];
    for (int base = 0; base < cnt; base += 64) {
      bool ok = base + lane < cnt;
      unsigned tl = 0, p = 0;
      if (ok) {
        if (li < 0) { const unsigned e = o2[base + lane]; tl = e >> 16; p = e & 0xFFFF; }
        else { const unsigned e = ((const unsigned*)(ws + WS_O1))[li * 2048 + base + lane]; ok = (int)(e >> 20) == ch; tl = (e >> 14) & 63; p = e & 0x3FFF; }
      }
      const v2u val = *(LAS v2u*)(p * 8);
      u64 m = __builtin_amdgcn_ballot_w64(ok);
      while (m) {
        const int i = __builtin_ctzll(m); m &= m - 1;
        const unsigned t = __builtin_amdgcn_readlane(tl, i), a = __builtin_amdgcn_readlane(val[0], i), c = __builtin_amdgcn_readlane(val[1], i);
        if ((unsigned)lane == t) { acc[0] += bflo(a); acc[1] += bfhi(a); acc[2] += bflo(c); acc[3] += bfhi(c); }
      }
    }
  }
  return acc;
}
DEVI unsigned pksafe(float a, float b) { return umin(pkbf(a, b), 0xFFFFFFFEu); }
DEVI v2u publish(v4f a, float d2, __amdgpu_buffer_rsrc_t rs, int soff, int lane) {
  const unsigned x0 = pksafe(a[0] * d2, a[1] * d2), x1 = pksafe(a[2] * d2, a[3] * d2);
  const unsigned y0 = dppu<0xF5>(x0), y1 = dppu<0xF5>(x1);
  if (!(lane & 1)) store16(((v4u){x0, x1, y0, y1}), rs, lane * 8, soff, 16);
  return (v2u){x0, x1};
}
DEVI unsigned max4(v4u v) { return umax(umax(v[0], v[1]), umax(v[2], v[3])); }
#define WAITVM0() __builtin_amdgcn_s_waitcnt(0x0F70)
#define WAITLG0() __builtin_amdgcn_s_waitcnt(0xC07F)
#define CFENCE() asm volatile("" ::: "memory")
constexpr int AUX_SC1 = 16;
constexpr int K3_LDS = SLB + 256;
#define STAMPL(i) do {} while (0)
#define XSLEEP 6
#define YD1 6
#define YD2 8
__global__ void __launch_bounds__(1024, 4) k_hops(char* ws) {
  extern __shared__ __attribute__((aligned(16))) char lds[];
  const int b = BID, slice = (b & 7) * 4 + (b >> 6), part = (b >> 3) & 7;
  const int tid = TID, wave = __builtin_amdgcn_readfirstlane(tid >> 6), lane = tid & 63;
  STAMPG(2, 0);
  STAMPL(0);
  const int c0 = part * 25 + wave, c1 = c0 + 16;
  const bool has1 = wave < 9;
  const __amdgpu_buffer_rsrc_t rs = mkrsrc(ws + WS_B, 9 * HOPB);
  const __amdgpu_buffer_rsrc_t rl = mkrsrc(ws + WS_LIST, NCH * 8192);
  const int l16 = lane * 16;
  constexpr int OOB = 0x40000000;
  const v4i m0 = *(const v4i*)(ws + WS_META + c0 * 16), m1 = *(const v4i*)(ws + WS_META + (has1 ? c1 : c0) * 16);
#pragma unroll
  for (int i = 0; i < 7; ++i)
    if (i < 6 || wave < 4)
      __builtin_amdgcn_global_load_lds((const GAS unsigned*)(ws + WS_B + (size_t)8 * HOPB + slice * SLB + (wave + 16 * i) * 1024 + l16), (LAS unsigned*)((wave + 16 * i) * 1024), 16, 0, 0);
  v4u L0[8];
#pragma unroll
  for (int i = 0; i < 4; ++i) L0[i] = __builtin_amdgcn_raw_buffer_load_b128(rl, l16, c0 * 8192 + i * 1024, 0);
  __builtin_amdgcn_sched_barrier(0);
  const int n0 = m0[0], n1 = has1 ? m1[0] : 0;
  const bool slow0 = (m0[1] | m0[2]) != 0, slow1 = (m1[1] | m1[2]) != 0;
#pragma unroll
  for (int i = 4; i < 8; ++i) L0[i] = __builtin_amdgcn_raw_buffer_load_b128(rl, l16, 4 * i < n0 ? c0 * 8192 + i * 1024 : OOB, 0);
  float d0 = ((const float*)(ws + WS_PDIS))[c0 * 64 + lane];
  float d1 = ((const float*)(ws + WS_PDIS))[(has1 ? c1 : c0) * 64 + (wave == 8 ? lane & 31 : lane)];
  v8bf sel;
  { const int g = lane >> 4, row = lane & 15;
#pragma unroll
    for (int j = 0; j < 8; ++j) sel[j] = (__bf16)(((row >> 2) == 2 * (g & 1) + (j >> 2) && (j & 1) == (row & 1)) ? 1.0f : 0.0f); }
  const int sidx = (lane & 2) ? 0xEEEE : 0x4444;
  int yo[4];
#pragma unroll
  for (int t = 0; t < 4; ++t) {
    const int xk = (t & 1) * 576 + tid, per = t < 2 ? 128 : 144, pj = xk / per;
    yo[t] = xk < 7 * per ? (((part + 1 + pj) & 7) * 25 + (t < 2 ? 16 : 20)) * 512 + (xk - pj * per) * 16 : OOB;
  }
  const int xo = ((part + wave - 8) & 7) * 25 * 512 + l16;
  WAITVM0();
  d0 *= d0; d1 *= d1;
  auto hops = [&](auto role) __attribute__((always_inline)) {
    constexpr bool HAS1 = decltype(role)::v;
    v4u L1[8];
    if constexpr (HAS1) {
#pragma unroll
      for (int i = 0; i < 8; ++i) L1[i] = __builtin_amdgcn_raw_buffer_load_b128(rl, l16, (4 * i < n1) ? c1 * 8192 + i * 1024 : OOB, 0);
    }
    WAITLG0();
    __builtin_amdgcn_s_barrier();
    STAMPL(1);
#pragma unroll 1
    for (int k = 1; k <= 7; ++k) {
      const int hb = k * HOPB + slice * SLB;
      if (k < 6 && (lane < 32 || HAS1))
        store16(((v4u){~0u, ~0u, ~0u, ~0u}), rs, l16 + (lane < 32 ? 0 : 16 * 512 - 512), hb + HOPB + c0 * 512, AUX_SC1);
      v2u r0, r1;
      v4u X[8];
      __builtin_amdgcn_s_setprio(2);
      {
        v4f a = gather(L0, n0, sel, sidx);
        if (slow0) a = slow_edges(a, ws, c0, m0[1], m0[2], lane);
        WAITVM0();
        r0 = publish(a, d0, rs, hb + c0 * 512, lane);
      }
      STAMPL(8 * k - 6);
      if constexpr (HAS1) {
        __builtin_amdgcn_s_setprio(0);
        v4f a = gather(L1, n1, sel, sidx);
        __builtin_amdgcn_s_setprio(2);
        if (slow1) a = slow_edges(a, ws, c1, m1[1], m1[2], lane);
        r1 = publish(a, d1, rs, hb + c1 * 512, lane);
        STAMPL(8 * k - 5);
        if (k == 7) break;
      } else {
        if (k == 7) break;
        if (XSLEEP) __builtin_amdgcn_s_sleep(XSLEEP);
        CFENCE();
#pragma unroll
        for (int i = 0; i < 4; ++i) X[i] = __builtin_amdgcn_raw_buffer_load_b128(rs, xo + i * 1024, hb, AUX_SC1);
        STAMPL(8 * k - 5);
      }
      __builtin_amdgcn_s_barrier();
      STAMPL(8 * k - 4);
      CFENCE();
      unsigned spins = 0, bad = 0;
      if constexpr (HAS1) {
        *(LAS v2u*)(c0 * 512 + lane * 8) = r0;
        *(LAS v2u*)(c1 * 512 + lane * 8) = r1;
        if (YD1) __builtin_amdgcn_s_sleep(YD1);
        CFENCE();
        X[0] = __builtin_amdgcn_raw_buffer_load_b128(rs, yo[0], hb, AUX_SC1);
        X[1] = __builtin_amdgcn_raw_buffer_load_b128(rs, yo[1], hb, AUX_SC1);
        if (YD2) __builtin_amdgcn_s_sleep(YD2);
        CFENCE();
        X[2] = __builtin_amdgcn_raw_buffer_load_b128(rs, yo[2], hb, AUX_SC1);
        X[3] = __builtin_amdgcn_raw_buffer_load_b128(rs, yo[3], hb, AUX_SC1);
        STAMPL(8 * k - 1);
#pragma unroll
        for (int t = 0; t < 4; ++t) {
          if (max4(X[t]) == ~0u) bad |= 1u << t;
          if (!(t & 1) || yo[t] != OOB) *(LAS v4u*)(yo[t]) = X[t];
          CFENCE();
        }
      } else {
#pragma unroll
        for (int i = 4; i < 8; ++i) X[i] = __builtin_amdgcn_raw_buffer_load_b128(rs, xo + i * 1024, hb, AUX_SC1);
        CFENCE();
        *(LAS v2u*)(c0 * 512 + lane * 8) = r0;
#pragma unroll
        for (int i = 0; i < 8; ++i) {
          if (max4(X[i]) == ~0u) bad |= 1u << i;
          *(LAS v4u*)(xo + i * 1024) = X[i];
          CFENCE();
        }
        STAMPL(8 * k - 1);
      }
      while (anyl(bad != 0)) {
        ++spins;
        if (bad) {
          const int i = __builtin_ctz(bad);
          const int o = HAS1 ? (i == 0 ? yo[0] : i == 1 ? yo[1] : i == 2 ? yo[2] : yo[3]) : xo + i * 1024;
          CFENCE();
          const v4u v = __builtin_amdgcn_raw_buffer_load_b128(rs, o, hb, AUX_SC1);
          if (max4(v) != ~0u || spins >= SPINCAP) { *(LAS v4u*)(o) = v; bad &= bad - 1; }
        }
      }
      STAMPL(8 * k - 3);
      WAITLG0();
      __builtin_amdgcn_s_barrier();
      STAMPL(8 * k - 2);
    }
  };
  if (has1) hops(IC<1>{}); else hops(IC<0>{});
}

constexpr int RF = 7;
constexpr int T_W = 0, T_SCR = (64 - RF) * 1024, T_HS = 12832, T_TOK = 163840 - 8 * T_HS, K4_LDS = 163840;
template <int I, int E, class F> DEVI void sfor(F&& f) { if constexpr (I < E) { f(IC<I>{}); sfor<I + 1, E>(f); } }
template <int RM, int BM> DEVI float dppmerge(float old, float x) { return u2f((unsigned)__builtin_amdgcn_update_dpp((int)f2u(old), (int)f2u(x), 0xE4, RM, BM, false)); }
DEVI float bperm(int byteaddr, float v) { return u2f((unsigned)__builtin_amdgcn_ds_bpermute(byteaddr, (int)f2u(v))); }
DEVI float vmax(float a, float b) { float r; asm("v_max_f32 %0, %1, %2" : "=v"(r) : "v"(a), "v"(b)); return r; }
DEVI float vmax32(float x) { v2u r = __builtin_amdgcn_permlane32_swap(f2u(x), f2u(x), false, false); return vmax(u2f(r[0]), u2f(r[1])); }
DEVI float swapadd16(float a, float b) { v2u r = __builtin_amdgcn_permlane16_swap(f2u(a), f2u(b), false, false); return u2f(r[0]) + u2f(r[1]); }
DEVI float swapadd32(float a, float b) { v2u r = __builtin_amdgcn_permlane32_swap(f2u(a), f2u(b), false, false); return u2f(r[0]) + u2f(r[1]); }
DEVI v8bf pack8(const v16f& a, int o) { return __builtin_bit_cast(v8bf, (v4u){pkbf(a[o], a[o + 1]), pkbf(a[o + 2], a[o + 3]), pkbf(a[o + 4], a[o + 5]), pkbf(a[o + 6], a[o + 7])}); }
#define TSTAMP(i) do {} while (0)
#define MFMA16(a, b, c) __builtin_amdgcn_mfma_f32_16x16x32_bf16(a, b, c, 0, 0, 0)
#define MFMA32(a, b, c) __builtin_amdgcn_mfma_f32_32x32x16_bf16(a, b, c, 0, 0, 0)
#define PIN() __builtin_amdgcn_sched_barrier(0x406)
DEVI void tf_batch(const int wave, const int nmask, const v8bf (&mreg)[RF], const v8bf (&xreg)[8], const float wmean, const float cc, const unsigned node2, const char* ws,
                   u16* __restrict__ out, const int lane) {
  constexpr int PD = 3;
  const int h = lane >> 5, n = (lane >> 3) & 3, hop = lane & 7;
  const v16f zero = {0.f, 0.f, 0.f, 0.f, 0.f, 0.f, 0.f, 0.f, 0.f, 0.f, 0.f, 0.f, 0.f, 0.f, 0.f, 0.f};
  LAS char* const wb = ldsbase(T_W + lane * 16);
  LAS char* const scw = ldsbase(T_SCR + wave * 128 + (lane & 31) * 4);
  LAS char* const scr = ldsbase(T_SCR + wave * 128 + n * 32 + h * 16);
  __builtin_amdgcn_s_setprio(1);
  v8bf go[8];
  {
    LAS char* const a0 = ldsbase(T_TOK + (7 - hop) * T_HS + h * 800 + wave * 32 + (n & nmask) * 8);
    LAS char* const a1 = ldsbase(T_TOK + (7 - hop) * T_HS + h * 800 + wave * 32 + (n & nmask) * 8 + 400);
#pragma unroll
    for (int s = 0; s < 8; ++s) {
      const v4bf lo = *(volatile LAS v4bf*)(a0 + s * 1600), hi = *(volatile LAS v4bf*)(a1 + s * 1600);
      go[s] = __builtin_shufflevector(lo, hi, 0, 1, 2, 3, 4, 5, 6, 7);
    }
  }
  PIN();
  TSTAMP(0);
  v4f xq = {0.f, 0.f, 0.f, 0.f};
  v4f gr0 = xq, gr1 = xq;
#pragma unroll
  for (int s = 0; s < 8; ++s) {
    const v4s lo = __builtin_bit_cast(v4s, __builtin_shufflevector(go[s], go[s], 0, 1, 2, 3)), hi = __builtin_bit_cast(v4s, __builtin_shufflevector(go[s], go[s], 4, 5, 6, 7));
    gr0 = __builtin_amdgcn_mfma_f32_4x4x4bf16_1k(lo, lo, gr0, 0, 0, 0);
    gr1 = __builtin_amdgcn_mfma_f32_4x4x4bf16_1k(hi, hi, gr1, 0, 0, 0);
    xq = MFMA16(xreg[s], go[s], xq);
    PIN();
  }
  PIN();
  TSTAMP(1);
  v8bf w[64], zb[4][2], pa[2];
  v16f acc[8], S;
  v4f rl, ga, gb, sc;
  float rho, t1, t3;
  unsigned p0, p1;
#pragma unroll
  for (int f = 0; f < RF; ++f) w[f] = mreg[f];
  sfor<0, 64>([&](auto fi) __attribute__((always_inline)) NOPK {
    constexpr int f = decltype(fi)::v;
    if constexpr (f + PD >= RF && f + PD < 64) w[f + PD] = *(LAS v8bf*)(wb + (f + PD - RF) * 1024);
    constexpr int T = f >> 3, s = f & 7;
    acc[T] = T < 4 ? MFMA32(w[f], go[s], s == 0 ? zero : acc[T]) : MFMA32(go[s], w[f], s == 0 ? zero : acc[T]);
    if constexpr (T > 0 && (s == 1 || s == 2)) {
      constexpr int U = T - 1, sg = s - 1;
      if constexpr (U < 4) S = MFMA32(go[2 * U + sg], pack8(acc[U], 8 * sg), (U == 0 && sg == 0) ? zero : S);
      else { zb[U - 4][sg] = pack8(acc[U], 8 * sg); asm volatile("" : "+v"(zb[U - 4][sg])); }
    }
    if constexpr (f == 3) {
      float e0 = gr0[0] + gr1[0], e1 = gr0[1] + gr1[1], e2 = gr0[2] + gr1[2], e3 = gr0[3] + gr1[3];
      asm("" : "+v"(e0), "+v"(e1), "+v"(e2), "+v"(e3));
      const float s2 = (lane & 2) ? ((lane & 1) ? e3 : e2) : ((lane & 1) ? e1 : e0);
      const float var = vmax(__builtin_fmaf(xq[0] * (-1.f / 128.f), xq[0], add32(s2)), 0.f) * (cc * cc * (1.f / 128.f));
      rho = cc * __builtin_amdgcn_rsqf(var + 1e-5f);
    }
    if constexpr (f == 30) { *(LAS float*)scw = rho; rl = *(LAS v4f*)scr; }
    if constexpr (f == 37) {
#pragma unroll
      for (int r = 0; r < 4; ++r) sc[r] = dppmerge<0xA, 0xC>(dppmerge<0xA, 0x3>(dppmerge<0x5, 0xC>(S[r], S[4 + r]), S[8 + r]), S[12 + r]) * rl[r];
      asm volatile("" : "+v"(sc));
      *(LAS float*)scw = xq[2]; ga = *(LAS v4f*)scr;
      *(LAS float*)scw = xq[3]; gb = *(LAS v4f*)scr;
    }
    if constexpr (f == 46) {
      const float m = vmax32(vmax(vmax(sc[0], sc[1]), vmax(sc[2], sc[3]))) * rho;
#pragma unroll
      for (int r = 0; r < 4; ++r) sc[r] = __builtin_amdgcn_exp2f(__builtin_fmaf(sc[r], rho, -m));
      const float inv = __builtin_amdgcn_rcpf(add32((sc[0] + sc[1]) + (sc[2] + sc[3])) * cc);
#pragma unroll
      for (int r = 0; r < 4; ++r) sc[r] *= rl[r] * inv;
      p0 = pkbf(sc[0], sc[1]); p1 = pkbf(sc[2], sc[3]);
      t1 = xq[0] + add32(__builtin_fmaf(sc[3], ga[3], __builtin_fmaf(sc[2], ga[2], __builtin_fmaf(sc[1], ga[1], sc[0] * ga[0]))));
      t3 = xq[1] + add32(__builtin_fmaf(sc[3], gb[3], __builtin_fmaf(sc[2], gb[2], __builtin_fmaf(sc[1], gb[1], sc[0] * gb[0]))));
      t3 = __builtin_fmaf(-t1, wmean, t3);
      asm volatile("" : "+v"(p0), "+v"(p1), "+v"(t1), "+v"(t3));
    }
    if constexpr (f == 15 || f == 31 || f == 47 || f == 63) TSTAMP(2 + (f >> 4));
    PIN();
  });
  zb[3][0] = pack8(acc[7], 0); zb[3][1] = pack8(acc[7], 8);
  const unsigned one = (hop >> 2) == h ? ((hop & 1) ? 0x3F800000u : 0x3F80u) : 0u;
  const unsigned d0 = (hop & 2) ? 0u : one, d1 = (hop & 2) ? one : 0u;
  v8bf da[2];
  pa[0] = __builtin_bit_cast(v8bf, (v4u){n == 0 ? p0 : 0u, n == 0 ? p1 : 0u, n == 1 ? p0 : 0u, n == 1 ? p1 : 0u});
  pa[1] = __builtin_bit_cast(v8bf, (v4u){n == 2 ? p0 : 0u, n == 2 ? p1 : 0u, n == 3 ? p0 : 0u, n == 3 ? p1 : 0u});
  da[0] = __builtin_bit_cast(v8bf, (v4u){n == 0 ? d0 : 0u, n == 0 ? d1 : 0u, n == 1 ? d0 : 0u, n == 1 ? d1 : 0u});
  da[1] = __builtin_bit_cast(v8bf, (v4u){n == 2 ? d0 : 0u, n == 2 ? d1 : 0u, n == 3 ? d0 : 0u, n == 3 ? d1 : 0u});
  LAS char* const tr0 = ldsbase(T_TOK + (7 - 4 * h - ((lane >> 2) & 3)) * T_HS + (((lane >> 4) & 1) * 4 + (lane & 3)) * 400 + wave * 32);
  LAS char* const tr1 = ldsbase(T_TOK + (7 - 4 * h - ((lane >> 2) & 3)) * T_HS + (((lane >> 4) & 1) * 4 + (lane & 3)) * 400 + wave * 32 + (2 & nmask) * 8);
  v8bf gt[4][2];
  auto rdgt = [&](int T) __attribute__((always_inline)) NOPK {
#pragma unroll
    for (int sg = 0; sg < 2; ++sg) {
      LAS char* const q = (sg ? tr1 : tr0) + T * 3200;
      gt[T][sg] = __builtin_shufflevector(__builtin_amdgcn_ds_read_tr16_b64_v4bf16((LAS v4bf*)q), __builtin_amdgcn_ds_read_tr16_b64_v4bf16((LAS v4bf*)(q + 8)), 0, 1, 2, 3, 4, 5, 6, 7);
    }
  };
  rdgt(0);
  PIN();
  v16f O[4], q;
#pragma unroll
  for (int T = 0; T < 4; ++T) {
    if (T < 3) rdgt(T + 1);
    PIN();
    O[T] = MFMA32(pa[0], zb[T][0], zero);
    O[T] = MFMA32(pa[1], zb[T][1], O[T]);
    O[T] = MFMA32(da[0], gt[T][0], O[T]);
    O[T] = MFMA32(da[1], gt[T][1], O[T]);
    if (T == 1) q = O[0] * O[0];
    if (T > 1) q += O[T - 1] * O[T - 1];
    if (T > 0) asm volatile("" : "+v"(q));
    PIN();
  }
  __builtin_amdgcn_s_setprio(0);
  TSTAMP(6);
  q += O[3] * O[3];
  float q8[8], q4[4], q2[2];
#pragma unroll
  for (int i = 0; i < 8; ++i) q8[i] = swapadd16(q[i], q[8 + i]);
  const bool b8 = lane & 8, b4 = lane & 4, b2 = lane & 2;
#pragma unroll
  for (int i = 0; i < 4; ++i) q4[i] = (b8 ? q8[4 + i] : q8[i]) + dppf<0x128>(b8 ? q8[i] : q8[4 + i]);
#pragma unroll
  for (int i = 0; i < 2; ++i) q2[i] = (b4 ? q4[2 + i] : q4[i]) + dppf<DPP_HM>(b4 ? q4[i] : q4[2 + i]);
  float qs = (b2 ? q2[1] : q2[0]) + dppf<DPP_X2>(b2 ? q2[0] : q2[1]);
  qs += dppf<DPP_X1>(qs);
  qs = bperm(128 * (hop >> 2) + 32 * n + 8 * (hop & 3), qs);
  TSTAMP(7);
  const float var = vmax(__builtin_fmaf(t1 * (-1.f / 128.f), t1, qs), 0.f) * (cc * cc * (1.f / 128.f));
  const float a = t3 * (cc * 1.4426950408889634f) * __builtin_amdgcn_rsqf(var + 1e-5f);
  float m = vmax(a, dppf<DPP_X1>(a)); m = vmax(m, dppf<DPP_X2>(m)); m = vmax(m, dppf<DPP_HM>(m));
  const float e = __builtin_amdgcn_exp2f(a - m);
  float sm = e + dppf<DPP_X1>(e); sm += dppf<DPP_X2>(sm); sm += dppf<DPP_HM>(sm);
  *(LAS float*)scw = e * cc * __builtin_amdgcn_rcpf(sm);
  LAS char* const gr = ldsbase(T_SCR + wave * 128 + h * 16);
  v4f gam[4];
#pragma unroll
  for (int k = 0; k < 4; ++k) gam[k] = *(LAS v4f*)(gr + 32 * k);
  float y[2][4];
#pragma unroll
  for (int T = 0; T < 4; ++T) {
    float v[4];
#pragma unroll
    for (int k = 0; k < 4; ++k) v[k] = __builtin_fmaf(gam[k][3], O[T][4 * k + 3], __builtin_fmaf(gam[k][2], O[T][4 * k + 2], __builtin_fmaf(gam[k][1], O[T][4 * k + 1], gam[k][0] * O[T][4 * k])));
    y[0][T] = swapadd32(v[0], v[1]);
    y[1][T] = swapadd32(v[2], v[3]);
  }
  TSTAMP(8);
  const unsigned node[2] = {node2 & 0xFFFFu, node2 >> 16};
#pragma unroll
  for (int k = 0; k < 2; ++k)
    if (node[k] < (unsigned)NN) {
      u16* o = out + node[k] * 128 + (__builtin_amdgcn_mbcnt_hi(~0u, __builtin_amdgcn_mbcnt_lo(~0u, 0)) & 31);
      const unsigned p01 = pkbf(y[k][0], y[k][1]), p23 = pkbf(y[k][2], y[k][3]);
      o[0] = (u16)p01; o[32] = (u16)(p01 >> 16); o[64] = (u16)p23; o[96] = (u16)(p23 >> 16);
    }
}
__global__ void NOPK __launch_bounds__(1024, 4) k_tf(const char* __restrict__ ws, u16* __restrict__ out) {
  extern __shared__ __attribute__((aligned(16))) char lds[];
  const int tid = TID, lane = tid & 63, wave = __builtin_amdgcn_readfirstlane(tid >> 6);
  STAMPG(3, 0);
  const int part = BID & 7, idx = BID >> 3;
  const int t0 = idx * 782 / 32, nt = (idx + 1) * 782 / 32 - t0;
  const int pos0 = part * 1600 + 2 * t0;
  {
    const int hop = wave >> 1;
#pragma unroll
    for (int t = 0; t < 7; ++t) {
      const int i = (wave & 1) + 2 * t, xk = i * 64 + lane;
      if (xk < 800) {
        const int s = xk / 25, u = xk - s * 25;
        __builtin_amdgcn_global_load_lds((const GAS unsigned*)(ws + WS_B + (size_t)hop * HOPB + s * SLB + pos0 * 8 + u * 16), (LAS unsigned*)(T_TOK + (7 - hop) * T_HS + i * 1024), 16, 0, 0);
      }
    }
  }
#pragma unroll
  for (int i = 0; i < 4; ++i) {
    const int blk = wave + 16 * i;
    if (blk < 64 - RF) __builtin_amdgcn_global_load_lds((const GAS unsigned*)(ws + WS_WF + (RF + blk) * 1024 + lane * 16), (LAS unsigned*)(T_W + blk * 1024), 16, 0, 0);
  }
  const bool work = 2 * wave < nt;
  v8bf mreg[RF], xreg[8];
  float cc = 1.f, wmean = 0.f;
  unsigned node[2] = {0xFFFFu, 0xFFFFu};
  const int nmask = 2 * wave + 1 < nt ? 3 : 1;
  if (work) {
#pragma unroll
    for (int f = 0; f < 8; ++f) xreg[f] = *(const v8bf*)(ws + WS_WF + 65536 + f * 1024 + lane * 16);
#pragma unroll
    for (int f = 0; f < RF; ++f) mreg[f] = *(const v8bf*)(ws + WS_WF + f * 1024 + lane * 16);
    wmean = *(const float*)(ws + WS_WF + 65536 + 8192);
    const int p = pos0 + 4 * wave;
    const float ca = ((const float*)(ws + WS_PCI))[p + ((lane >> 3) & nmask)];
    cc = (lane & 7) ? ca : 1.f;
    node[0] = ((const u16*)(ws + WS_INV))[p + (lane >> 5)];
    if (nmask == 3) node[1] = ((const u16*)(ws + WS_INV))[p + 2 + (lane >> 5)];
  }
  asm volatile("s_waitcnt vmcnt(0)" ::: "memory");
  wgsync();
  STAMPG(3, 1); STAMPV(3, 60, __builtin_amdgcn_s_memtime());
  if (work) tf_batch(wave, nmask, mreg, xreg, wmean, cc, node[0] | (node[1] << 16), ws, out, lane);
  STAMPG(3, 2); STAMPV(3, 61, __builtin_amdgcn_s_memtime());
}

extern "C" void kernel_launch(void* const* d_in, const int* in_sizes, int n_in, void* d_out, int out_size, void* d_ws, size_t ws_size, hipStream_t stream) {
  (void)in_sizes; (void)n_in; (void)out_size; (void)ws_size;
  char* ws = (char*)d_ws;
  (void)hipFuncSetAttribute((const void*)k_prep, hipFuncAttributeMaxDynamicSharedMemorySize, K1_LDS);
  (void)hipFuncSetAttribute((const void*)k_lists, hipFuncAttributeMaxDynamicSharedMemorySize, K2_LDS);
  (void)hipFuncSetAttribute((const void*)k_hops, hipFuncAttributeMaxDynamicSharedMemorySize, K3_LDS);
  (void)hipFuncSetAttribute((const void*)k_tf, hipFuncAttributeMaxDynamicSharedMemorySize, K4_LDS);
  k_prep<<<G1 + 8, 1024, K1_LDS, stream>>>((const u16*)d_in[0], (const int*)d_in[1], (const int*)d_in[2], (const u16*)d_in[3],
                                                   (const u16*)d_in[4], (const u16*)d_in[5], (const u16*)d_in[6], (const u16*)d_in[7], (const u16*)d_in[8], ws);
  k_lists<<<NCH + 5, 1024, K2_LDS, stream>>>(ws, (const u16*)d_in[8]);
  k_hops<<<256, 1024, K3_LDS, stream>>>(ws);
  k_tf<<<256, 1024, K4_LDS, stream>>>(ws, (u16*)d_out);
}
```

```cpp
#include <hip/hip_runtime.h>
#include <stdint.h>

typedef unsigned short u16;
typedef unsigned char u8;
typedef unsigned long long u64;
typedef __bf16 v8bf __attribute__((ext_vector_type(8)));
typedef __bf16 v4bf __attribute__((ext_vector_type(4)));
typedef __bf16 v2bf __attribute__((ext_vector_type(2)));
typedef float v4f __attribute__((ext_vector_type(4)));
typedef float v2f __attribute__((ext_vector_type(2)));
typedef float v16f __attribute__((ext_vector_type(16)));
typedef unsigned v4u __attribute__((ext_vector_type(4)));
typedef unsigned v2u __attribute__((ext_vector_type(2)));
typedef int v4i __attribute__((ext_vector_type(4)));
typedef short v4s __attribute__((ext_vector_type(4)));
typedef u16 v2h __attribute__((ext_vector_type(2)));
#define LAS __attribute__((address_space(3)))
#define GAS __attribute__((address_space(1)))
#if defined(__HIP_DEVICE_COMPILE__)
#define NOPK __attribute__((target("no-packed-fp32-ops")))
#else
#define NOPK
#endif
#define DEVI __device__ __forceinline__ NOPK
#define TID ((int)__builtin_amdgcn_workitem_id_x())
#define BID ((int)__builtin_amdgcn_workgroup_id_x())
DEVI float u2f(unsigned u) { return __builtin_bit_cast(float, u); }
DEVI unsigned f2u(float f) { return __builtin_bit_cast(unsigned, f); }
DEVI unsigned umin(unsigned a, unsigned b) { return a < b ? a : b; }
DEVI unsigned umax(unsigned a, unsigned b) { return a > b ? a : b; }
DEVI void wgsync() { __builtin_amdgcn_fence(__ATOMIC_RELEASE, "workgroup"); __builtin_amdgcn_s_barrier(); __builtin_amdgcn_fence(__ATOMIC_ACQUIRE, "workgroup"); }

constexpr int NN = 12500, NE = 400000;
constexpr int NCH = 200;
constexpr int NPOS = NCH * 64;
constexpr int SLB = NPOS * 8;
constexpr int HOPB = 32 * SLB;
constexpr int G1 = 248, CAP = 32, EPW = 807, NODEWG = 196;
constexpr int SEGB = G1 * CAP * 4;
constexpr size_t WS_B = 0;
constexpr size_t WS_SEG = WS_B + (size_t)9 * HOPB;
constexpr size_t WS_LIST = WS_SEG + (size_t)NCH * SEGB;
constexpr size_t WS_META = WS_LIST + (size_t)NCH * 8192;
constexpr int H_META = NCH * 8192, H_FLAG = H_META + NCH * 128;
constexpr int HANDB = H_FLAG + 7 * 256 * 128;
constexpr size_t WS_WF = WS_LIST + HANDB;
constexpr size_t WS_RS = WS_WF + 65536;
constexpr size_t WS_INV = WS_RS + 12288;
constexpr size_t WS_PDIS = WS_INV + 25600;
constexpr size_t WS_PCI = WS_PDIS + 51200;
constexpr size_t WS_O1C = WS_PCI + 51200;
constexpr size_t WS_O1 = WS_O1C + 1024;
constexpr size_t WS_O2 = WS_O1 + (size_t)G1 * 2048 * 4;
constexpr size_t WS_DBG = WS_O2 + (size_t)NCH * SEGB;

#define SPINCAP (1u << 22)
#define STAMPV(kern, i, v) do {} while (0)
#define STAMPA(kern, i) do {} while (0)
#define STAMPG(kern, i) do {} while (0)
#define STAMPC(i) do {} while (0)
#define STAMPS3(x)

DEVI LAS char* ldsbase(int a) { asm volatile("" : "+v"(a)); return (LAS char*)a; }
DEVI float bflo(unsigned u) { return u2f(u << 16); }
DEVI float bfhi(unsigned u) { return u2f(u & 0xffff0000u); }
DEVI unsigned pkbf(float a, float b) { v2f v = {a, b}; return __builtin_bit_cast(unsigned, __builtin_convertvector(v, v2bf)); }
DEVI __amdgpu_buffer_rsrc_t mkrsrc(const void* p, unsigned bytes) { return __builtin_amdgcn_make_buffer_rsrc((void*)p, 0, bytes, 0x00020000); }
#define store16(v, rs, voff, soff, aux) __builtin_amdgcn_raw_buffer_store_b128(v, rs, (voff) + (soff), 0, aux)
template <int C> DEVI unsigned dppu(unsigned x) { return (unsigned)__builtin_amdgcn_update_dpp(0, (int)x, C, 0xF, 0xF, true); }
template <int C> DEVI float dppf(float x) { return u2f(dppu<C>(f2u(x))); }
constexpr int DPP_X1 = 0xB1, DPP_X2 = 0x4E, DPP_HM = 0x141, DPP_RM = 0x140;
DEVI float add16(float x) { v2u r = __builtin_amdgcn_permlane16_swap(f2u(x), f2u(x), false, false); return u2f(r[0]) + u2f(r[1]); }
DEVI float add32(float x) { v2u r = __builtin_amdgcn_permlane32_swap(f2u(x), f2u(x), false, false); return u2f(r[0]) + u2f(r[1]); }
DEVI unsigned or16(unsigned x) { v2u r = __builtin_amdgcn_permlane16_swap(x, x, false, false); return r[0] | r[1]; }

DEVI bool anyl(bool c) { return __builtin_amdgcn_ballot_w64(c) != 0; }
constexpr unsigned LGRAN = 4;
template <int I> struct IC { static constexpr int v = I; };
DEVI unsigned key2(unsigned d, unsigned idx2) {
  v2h v = __builtin_bit_cast(v2h, d);
  v2h t = __builtin_elementwise_sub_sat((v2h){0x3F80, 0x3F80}, v);
  t = __builtin_elementwise_min(t, (v2h){4095, 4095});
  return (__builtin_bit_cast(unsigned, t) << 3) | idx2;
}
DEVI v4u keys8(v4u d) { return (v4u){key2(d[0], 0x00010000u), key2(d[1], 0x00030002u), key2(d[2], 0x00050004u), key2(d[3], 0x00070006u)}; }
DEVI unsigned rank8(v4u k, unsigned own) {
  const v2h o = __builtin_bit_cast(v2h, own | (own << 16));
  unsigned t[4];
#pragma unroll
  for (int i = 0; i < 4; ++i) { const unsigned ki = k[i]; const v2h df = __builtin_bit_cast(v2h, ki) - o; t[i] = __builtin_bit_cast(unsigned, df); }
  const unsigned a = __builtin_amdgcn_perm(t[0], t[1], 0x07050301u) & 0x80808080u;
  const unsigned b = __builtin_amdgcn_perm(t[2], t[3], 0x07050301u) & 0x80808080u;
  return __builtin_popcount(a) + __builtin_popcount(b);
}
DEVI unsigned chunk_of(unsigned w, unsigned r) {
  const u64 t0 = 0x150D0E09100C0800ull, t1 = 0x1611120A14020504ull, t2 = 0x17130F0B06070301ull;
  const unsigned j = w >> 3;
  const u64 t = j == 0 ? t0 : j == 1 ? t1 : t2;
  const unsigned slot = j == 3 ? 24u : (unsigned)(t >> (8 * r)) & 31u;
  return ((w + r) & 7) * 25 + slot;
}

#define K1AUX 0
constexpr int L1_LUT = 0, L1_CNT = 512, L1_ROW = L1_CNT + 1024, L1_D = L1_ROW + 64, L1_KEY = 2048, L1_SEG = L1_KEY + 25088, L1_X = L1_SEG + NCH * CAP * 4, K1_LDS = L1_X + 64 * 272;
DEVI unsigned pos_of(unsigned n) {
  const unsigned r = rank8(*(LAS v4u*)(L1_KEY + (n >> 3) * 16), *(LAS u16*)(L1_KEY + n * 2));
  return *(LAS u16*)(L1_LUT + ((n >> 9) * 8 + r) * 2) | ((n >> 3) & 63);
}
#define LDS_ADD(off, v) __hip_atomic_fetch_add((LAS unsigned*)(off), v, __ATOMIC_RELAXED, __HIP_MEMORY_SCOPE_WORKGROUP)
#define LDS_OR(off, v) (void)__hip_atomic_fetch_or((LAS unsigned*)(off), v, __ATOMIC_RELAXED, __HIP_MEMORY_SCOPE_WORKGROUP)
__global__ void __launch_bounds__(1024, 4) k_prep(const u16* __restrict__ x, const int* __restrict__ src, const int* __restrict__ dst, const u16* __restrict__ dis,
                                                  const u16* __restrict__ wq, const u16* __restrict__ wk, const u16* __restrict__ wv, const u16* __restrict__ wo, const u16* __restrict__ wr, char* __restrict__ ws) {
  extern __shared__ __attribute__((aligned(16))) char lds[];
  const int tid = TID, b = BID;
  STAMPA(0, 0);
  if (b < G1) {
    const __amdgpu_buffer_rsrc_t rdis = mkrsrc(dis, NN * 2), rx = mkrsrc(x, NN * 256), rb = mkrsrc(ws + WS_B, 9 * HOPB), rsg = mkrsrc(ws + WS_SEG, NCH * SEGB);
    const int q = b * EPW + tid, n0 = b * 64, t16 = tid * 16;
    const bool act = tid < EPW && q < NE / 2;
    v2u s2 = {0, 0}, d2 = {0, 0};
    if (act) { d2 = ((const v2u*)dst)[q]; s2 = ((const v2u*)src)[q]; }
    const v4u kd0 = __builtin_amdgcn_raw_buffer_load_b128(rdis, t16, 0, 0), kd1 = __builtin_amdgcn_raw_buffer_load_b128(rdis, t16, 16384, 0);
    const v4u xv = __builtin_amdgcn_raw_buffer_load_b128(rx, t16, n0 * 256, 0);
    const unsigned dn = __builtin_amdgcn_raw_buffer_load_b32(rdis, (tid & 62) * 2, n0 * 2, 0);
    {
      const v4u s = {~0u, ~0u, ~0u, ~0u};
      if (b < HOPB / 16384) store16(s, rb, t16, HOPB + b * 16384, 16);
      if (tid < 512 && (b * 512 + tid) * 16 < HANDB) *(v4u*)(ws + WS_LIST + (b * 512 + tid) * 16) = s;
      *(LAS v4u*)(L1_SEG + t16) = s;
      if (tid < NCH * CAP / 4 - 1024) *(LAS v4u*)(L1_SEG + 16384 + t16) = s;
    }
    if (tid < 256) { *(LAS unsigned*)(L1_CNT + tid * 4) = 0; *(LAS u16*)(L1_LUT + tid * 2) = (u16)(chunk_of(tid >> 3, tid & 7) * 64); }
    *(LAS v4u*)(L1_KEY + t16) = keys8(kd0);
    if (tid < 1568 - 1024) *(LAS v4u*)(L1_KEY + 16384 + t16) = keys8(kd1);
    __syncthreads();
    STAMPG(0, 1);
    if (act) {
      unsigned pd[2], ps[2], sl[2];
#pragma unroll
      for (int i = 0; i < 2; ++i) { pd[i] = pos_of(d2[i]); sl[i] = LDS_ADD(L1_CNT + (pd[i] >> 6) * 4, 1u); }
#pragma unroll
      for (int i = 0; i < 2; ++i) ps[i] = pos_of(s2[i]);
#pragma unroll
      for (int i = 0; i < 2; ++i) *(LAS unsigned*)(sl[i] < CAP ? L1_SEG + (pd[i] >> 6) * (CAP * 4) + sl[i] * 4 : L1_CNT + 1016) = ((pd[i] & 63) << 16) | ps[i];
      if (umax(sl[0], sl[1]) >= CAP) {
#pragma unroll
        for (int i = 0; i < 2; ++i)
          if (sl[i] >= CAP) ((unsigned*)(ws + WS_O1))[b * 2048 + LDS_ADD(L1_CNT + 1020, 1u)] = (pd[i] << 14) | ps[i];
      }
    }
    if (b < NODEWG) {
      *(LAS v4u*)(L1_X + (tid >> 4) * 272 + (tid & 15) * 16) = xv;
      if (tid >= 960) {
        const int i = tid & 63, n = n0 + i;
        const unsigned r = rank8(*(LAS v4u*)(L1_KEY + (n >> 3) * 16), *(LAS u16*)(L1_KEY + n * 2));
        const unsigned pos = chunk_of(n0 >> 9, r) * 64 + ((n >> 3) & 63);
        const float dv = (i & 1) ? bfhi(dn) : bflo(dn);
        *(LAS u8*)(L1_ROW + r * 8 + (i >> 3)) = (u8)i;
        *(LAS float*)(L1_D + (r * 8 + (i >> 3)) * 4) = dv;
        ((u16*)(ws + WS_INV))[pos] = n < NN ? (u16)n : (u16)0xFFFF;
        ((float*)(ws + WS_PDIS))[pos] = dv;
        ((float*)(ws + WS_PCI))[pos] = dv == 0.f ? 1.f : 1.f / dv;
      }
    }
    __syncthreads();
    STAMPG(0, 2);
    {
      const int so = (tid >> 3) * SEGB + (tid & 7) * 16 + b * (CAP * 4);
      store16(*(LAS v4u*)(L1_SEG + t16), rsg, so, 0, K1AUX);
      if (tid < NCH * CAP / 4 - 1024) store16(*(LAS v4u*)(L1_SEG + 16384 + t16), rsg, so, 128 * SEGB, K1AUX);
    }
    if (tid == 0) ((unsigned*)(ws + WS_O1C))[b] = *(LAS unsigned*)(L1_CNT + 1020);
    if (b < NODEWG) {
      const int u = tid & 3, s = (tid >> 2) & 31, r = __builtin_amdgcn_readfirstlane(tid >> 7);
      const unsigned rr = *(LAS u16*)(L1_ROW + r * 8 + 2 * u);
      const v2f dd = *(LAS v2f*)(L1_D + r * 32 + u * 8);
      const v2u a = *(LAS v2u*)(L1_X + (rr & 0xff) * 272 + s * 8), c = *(LAS v2u*)(L1_X + (rr >> 8) * 272 + s * 8);
      const unsigned off = s * SLB + u * 16 + (chunk_of(n0 >> 9, r) * 64 + ((n0 >> 3) & 63)) * 8;
      store16(((v4u){a[0], a[1], c[0], c[1]}), rb, off, 0, K1AUX);
      store16(((v4u){pkbf(bflo(a[0]) * dd[0], bfhi(a[0]) * dd[0]), pkbf(bflo(a[1]) * dd[0], bfhi(a[1]) * dd[0]), pkbf(bflo(c[0]) * dd[1], bfhi(c[0]) * dd[1]), pkbf(bflo(c[1]) * dd[1], bfhi(c[1]) * dd[1])}),
              rb, off, 8 * HOPB, K1AUX);
    }
    STAMPA(0, 4);
  } else {
    const int v = b - G1, which = __builtin_amdgcn_readfirstlane(tid >> 9), rt = (tid >> 6) & 7, lane = tid & 63, g = lane >> 4, c = lane & 15;
    const int e = 16 * v + (which == 0 ? (c & 3) | ((c & 4) << 1) | ((c & 8) >> 1) : c);
    const u16* wa = which == 0 ? wq : wv;
    v8bf af[4], bf[4];
#pragma unroll
    for (int ks = 0; ks < 4; ++ks) af[ks] = *(const v8bf*)(wa + (16 * rt + c) * 128 + 32 * ks + 8 * g);
    const float we = bflo(wr[e]);
    if (which == 0) {
#pragma unroll
      for (int ks = 0; ks < 4; ++ks) bf[ks] = *(const v8bf*)(wk + e * 128 + 32 * ks + 8 * g);
    } else {
      unsigned t[32];
#pragma unroll
      for (int i = 0; i < 32; ++i) t[i] = wo[(32 * (i >> 3) + 8 * g + (i & 7)) * 128 + e];
#pragma unroll
      for (int ks = 0; ks < 4; ++ks) bf[ks] = __builtin_bit_cast(v8bf, (v4u){t[8 * ks] | (t[8 * ks + 1] << 16), t[8 * ks + 2] | (t[8 * ks + 3] << 16), t[8 * ks + 4] | (t[8 * ks + 5] << 16), t[8 * ks + 6] | (t[8 * ks + 7] << 16)});
    }
    v4f acc = {0, 0, 0, 0};
#pragma unroll
    for (int ks = 0; ks < 4; ++ks) acc = __builtin_amdgcn_mfma_f32_16x16x32_bf16(af[ks], bf[ks], acc, 0, 0, 0);
    const float cs = add32(add16((acc[0] + acc[1]) + (acc[2] + acc[3])));
    float* csL = (float*)lds;
    if (lane < 16) csL[(which * 16 + c) * 8 + rt] = cs;
    __syncthreads();
    const v4f p0 = *(const v4f*)(csL + (which * 16 + c) * 8), p1 = *(const v4f*)(csL + (which * 16 + c) * 8 + 4);
    acc -= (((p0[0] + p0[1]) + (p0[2] + p0[3])) + ((p1[0] + p1[1]) + (p1[2] + p1[3]))) * (1.f / 128.f);
    if (which == 0) acc *= 1.4426950408889634f * 0.08838834764831845f;
    *(v2u*)(ws + WS_WF + which * 32768 + (((v >> 1) * 8 + rt) * 64 + 32 * (g >> 1) + 16 * (v & 1) + c) * 16 + 8 * (g & 1)) = (v2u){pkbf(acc[0], acc[1]), pkbf(acc[2], acc[3])};
    v4f t;
#pragma unroll
    for (int r = 0; r < 4; ++r) {
      const float u = acc[r] * we;
      float x0 = (c & 1) ? u : acc[r];
      x0 += dppf<DPP_X1>((c & 1) ? acc[r] : u); x0 += dppf<DPP_X2>(x0); x0 += dppf<0x124>(x0); x0 += dppf<0x128>(x0);
      t[r] = x0;
    }
    if (c <= which) *(v4f*)(ws + WS_RS + (((which + c) * 8 + v) * 128 + 16 * rt + 4 * g) * 4) = t;
    STAMPA(0, 9);
  }
}

constexpr int L2_CNT = SLB, L2_MSK = L2_CNT + 8192, L2_OUT = L2_MSK + 2048 + 64, L2_PICK = L2_OUT + 8192, L2_EXT = L2_PICK + 16384, EXTCAP = 4096;
constexpr int M_E2 = L2_MSK + 512, M_BK = L2_MSK + 768, M_DG = L2_MSK + 1024, M_LO = L2_MSK + 1280, M_FR = L2_MSK + 1536, M_OV = L2_MSK + 2048;
constexpr int RF = 9;
constexpr int T_W = 0, T_SCR = (64 - RF) * 1024, T_X = T_SCR + 13 * 256, T_HS = 12832, T_TOK = 163840 - 8 * T_HS, T_PARK = SLB, KM_LDS = 163840;
static_assert(T_X + 1284 <= T_TOK && T_PARK + RF * 1024 <= T_TOK + 4 * T_HS && T_PARK >= T_TOK + 3 * T_HS && L2_EXT + EXTCAP * 4 <= KM_LDS, "LDS map");
constexpr int H_SYNC = SLB + RF * 1024;
static_assert(H_SYNC == M_DG, "free when it is set");
constexpr int LDS_NONE = 0x7FFF0;
constexpr int OOB = 0x40000000;
constexpr int AUX_SC1 = 16;
#define WAITVM0() __builtin_amdgcn_s_waitcnt(0x0F70)
#define WAITLG0() __builtin_amdgcn_s_waitcnt(0xC07F)
#define CFENCE() asm volatile("" ::: "memory")
#define LBAR() do { CFENCE(); WAITLG0(); __builtin_amdgcn_s_barrier(); CFENCE(); } while (0)
#define load16(rs, voff, soff, aux) __builtin_amdgcn_raw_buffer_load_b128(rs, voff, soff, aux)
DEVI unsigned max4(v4u v) { return umax(umax(v[0], v[1]), umax(v[2], v[3])); }
DEVI unsigned now() { return (unsigned)__builtin_amdgcn_s_memrealtime() & 0x7FFFFFFFu; }
DEVI int tdiff(unsigned a, unsigned b) { return (int)((a - b) << 1) >> 1; }
DEVI void wait_until(unsigned t) {
#pragma unroll 1
  for (int i = 0; i < 256 && tdiff(now(), t) < 0; ++i) __builtin_amdgcn_s_sleep(1);
  CFENCE();
}
DEVI int wave_max(int v) {
  v = v > (int)dppu<DPP_X1>(v) ? v : (int)dppu<DPP_X1>(v); v = v > (int)dppu<DPP_X2>(v) ? v : (int)dppu<DPP_X2>(v);
  v = v > (int)dppu<DPP_HM>(v) ? v : (int)dppu<DPP_HM>(v); v = v > (int)dppu<DPP_RM>(v) ? v : (int)dppu<DPP_RM>(v);
  const int a = __builtin_amdgcn_readlane(v, 0), c = __builtin_amdgcn_readlane(v, 16);
  return a > c ? a : c;
}
DEVI void dma1k(const char* gsrc, int ldsdst) {
  unsigned keep;
  asm volatile("s_mov_b32 %0, m0\n\ts_mov_b32 m0, %2\n\ts_nop 0\n\tglobal_load_lds_dwordx4 %1, off\n\ts_mov_b32 m0, %0" : "=&s"(keep) : "v"(gsrc), "s"(ldsdst) : "memory");
}
DEVI int kth_high(v2u m, unsigned k) {
  const unsigned ch = __builtin_popcount(m[1]);
  const bool hi = k < ch;
  unsigned w = hi ? m[1] : m[0];
  k = hi ? k : k - ch;
  if (k >= (unsigned)__builtin_popcount(w)) return -1;
  int pos = hi ? 32 : 0;
#pragma unroll
  for (int sh = 16; sh >= 1; sh >>= 1) {
    const unsigned up = w >> sh, cu = __builtin_popcount(up);
    const bool t = k < cu;
    w = t ? up : w & ((1u << sh) - 1u);
    k = t ? k : k - cu;
    pos += t ? sh : 0;
  }
  return pos;
}

DEVI void build_lists(char* ws, const int ch, const int slice, const int tid, const int lane, const int wave) {
  const __amdgpu_buffer_rsrc_t rh = mkrsrc(ws + WS_LIST, HANDB), ro = mkrsrc(ws + WS_O2 + (size_t)ch * SEGB, SEGB);
  v4u e4[2] = {{~0u, ~0u, ~0u, ~0u}, {~0u, ~0u, ~0u, ~0u}};
  if (tid < G1 * 4) {
    const v4u* seg = (const v4u*)(ws + WS_SEG + (size_t)ch * SEGB) + (tid >> 2) * 8 + (tid & 3);
    e4[0] = seg[0]; e4[1] = seg[4];
  }
  const unsigned o1c = tid < G1 ? ((const unsigned*)(ws + WS_O1C))[tid] : 0u;
  if (tid < (L2_OUT - L2_CNT) / 16) *(LAS v4u*)(L2_CNT + tid * 16) = (v4u){0, 0, 0, 0};
  if (tid >= 512) *(LAS v4u*)(L2_OUT - 8192 + tid * 16) = (v4u){0xFFFEFFFEu, 0xFFFEFFFEu, 0xFFFEFFFEu, 0xFFFEFFFEu};
  LBAR();
  STAMPG(1, 1);
  const bool more = anyl(e4[1][0] != ~0u);
#pragma unroll
  for (int i = 0; i < 8; ++i)
    if (i < 4 || more) {
      const unsigned e = e4[i >> 2][i & 3], ln = e >> 16;
      e4[i >> 2][i & 3] = e | (umin(LDS_ADD(L2_CNT + ((e ^ ln) & 31) * 256 + ln * 4, 1u), 3u) << 22);
    }
  if (o1c) *(LAS unsigned*)(M_OV + 4) = 1;
  LBAR();
  STAMPG(1, 2);
  auto place4 = [&](const v4u ev) __attribute__((always_inline)) NOPK {
    unsigned off[4], tot = 0;
#pragma unroll
    for (int i = 0; i < 4; ++i) {
      const u64 m = __builtin_amdgcn_ballot_w64((ev[i] >> 16) - 64u < 192u);
      off[i] = __builtin_amdgcn_mbcnt_hi((unsigned)(m >> 32), __builtin_amdgcn_mbcnt_lo((unsigned)m, tot));
      tot += __builtin_popcountll(m);
    }
    unsigned base;
    asm volatile("ds_add_rtn_u32 %0, %1, %2\n s_waitcnt lgkmcnt(0)" : "=&v"(base) : "v"(lane == 0 ? M_OV + 12 : LDS_NONE), "v"(tot) : "memory");
    base = __builtin_amdgcn_readfirstlane(base);
#pragma unroll
    for (int i = 0; i < 4; ++i) {
      const unsigned e = ev[i], ln = e >> 16, d = (e ^ ln) & 31;
      *(LAS u16*)(ln < 64 ? L2_OUT + (d >> 3) * 1024 + ln * 16 + (d & 7) * 2 : LDS_NONE) = (u16)e;
      *(LAS unsigned*)(ln - 64u < 192u && base + off[i] < EXTCAP ? L2_EXT + (base + off[i]) * 4 : LDS_NONE) = e;
    }
    if (base + tot > EXTCAP) {
#pragma unroll
      for (int i = 0; i < 4; ++i)
        if ((ev[i] >> 16) - 64u < 192u && base + off[i] >= EXTCAP) __builtin_amdgcn_raw_buffer_store_b32(ev[i] & 0x3FFFFFu, ro, LDS_ADD(M_OV, 1u) * 4, 0, AUX_SC1);
    }
  };
  if (wave < 2) { place4(e4[0]); if (more) place4(e4[1]); }
  else if (wave >= 8) {
    const int d = 4 * (wave - 8);
    unsigned u0 = 0, u1 = 0, u2 = 0, dg = 0;
#pragma unroll
    for (int i = 0; i < 4; ++i) {
      const unsigned n = *(LAS unsigned*)(L2_CNT + (d + i) * 256 + lane * 4);
      dg += n;
      u0 |= umin(n, 1u) << i; u1 |= (n > 1 ? 1u : 0u) << i; u2 |= (n > 2 ? 1u : 0u) << i;
    }
    (void)__hip_atomic_fetch_or((LAS u64*)(L2_MSK + lane * 8), (u64)(u0 << d) | ((u64)(u1 << d) << 32), __ATOMIC_RELAXED, __HIP_MEMORY_SCOPE_WORKGROUP);
    LDS_OR(M_E2 + lane * 4, u2 << d);
    LDS_OR(M_BK + (lane ^ d) * 4, ((u0 & 1u) | (dppu<DPP_X1>(u0) & 2u) | (dppu<DPP_X2>(u0) & 4u) | (dppu<0x1B>(u0) & 8u)) << d);
    (void)LDS_ADD(M_DG + lane * 4, dg);
  }
  LBAR();
  STAMPG(1, 3);
  if (wave < 2) {
    __builtin_amdgcn_s_setprio(3);
    unsigned lf, bf, ex;
    if (wave == 0) { const v2u t = *(LAS v2u*)(L2_MSK + lane * 8); lf = ~t[0]; ex = t[1]; bf = ~*(LAS unsigned*)(M_BK + lane * 4); }
    else {
      unsigned dg = *(LAS unsigned*)(M_DG + lane * 4);
      ex = *(LAS unsigned*)(M_E2 + lane * 4);
      dg = umax(dg, dppu<DPP_X1>(dg)); dg = umax(dg, dppu<DPP_X2>(dg)); dg = umax(dg, dppu<DPP_HM>(dg)); dg = umax(dg, dppu<DPP_RM>(dg));
      const unsigned m = umax(umax((unsigned)__builtin_amdgcn_readlane(dg, 0), (unsigned)__builtin_amdgcn_readlane(dg, 16)), umax((unsigned)__builtin_amdgcn_readlane(dg, 32), (unsigned)__builtin_amdgcn_readlane(dg, 48)));
      const unsigned L = umin(umax((m + LGRAN - 1) & ~(LGRAN - 1u), 32u), 64u);
      if (lane == 0) { *(LAS unsigned*)(M_OV + 8) = L; __builtin_amdgcn_raw_buffer_store_b64(((v2u){L >> 1, now()}), rh, H_META + ch * 128, 0, AUX_SC1); }
      lf = bf = (unsigned)((1ull << (L - 32)) - 1ull);
    }
    LAS unsigned* pk = (LAS unsigned*)ldsbase(L2_PICK + wave * 8192 + lane * 4);
    int d = 0;
#pragma unroll
    for (int i = 0; i < 32; ++i) {
      if (i) {
        const int t = __builtin_ctz(i);
        if (t == 0) { bf = dppu<DPP_X1>(bf); d ^= 1; }
        else if (t == 1) { bf = dppu<DPP_X2>(bf); d ^= 2; }
        else if (t == 2) { bf = dppu<DPP_HM>(bf); d ^= 7; }
        else if (t == 3) { bf = dppu<DPP_RM>(bf); d ^= 15; }
        else { const v2u r = __builtin_amdgcn_permlane16_swap(bf, bf, false, false); bf = (lane & 16) ? r[0] : r[1]; d ^= 16; }
      }
      const unsigned f0 = lf & bf;
      const unsigned p = f0 & ~(f0 - ((ex >> d) & 1u));
      lf ^= p; bf ^= p;
      pk[d * 64] = p;
    }
    *(LAS unsigned*)(M_FR + lane * 8 + wave * 4) = lf;
    __builtin_amdgcn_s_setprio(0);
  } else {
    place4(e4[0]); if (more) place4(e4[1]);
    const char* src = ws + WS_B + (size_t)8 * HOPB + slice * SLB + lane * 16;
#pragma unroll 1
    for (int i = wave - 2; i < 100; i += 14) dma1k(src + i * 1024, i * 1024);
  }
  LBAR();
  STAMPG(1, 4);
  const unsigned nex = umin(*(LAS unsigned*)(M_OV + 12), EXTCAP);
  for (unsigned i = tid; i < nex; i += 1024) {
    const unsigned e = *(LAS unsigned*)(L2_EXT + i * 4), rk = e >> 22, ln = (e >> 16) & 63, d = (e ^ ln) & 31;
    const unsigned m = *(LAS unsigned*)(rk < 3 ? L2_PICK - 8192 + rk * 8192 + d * 256 + ln * 4 : LDS_NONE);
    int c;
    if (m) c = __builtin_ctz(m) + 32 * rk - 32;
    else c = kth_high(*(LAS v2u*)(M_FR + ln * 8), LDS_ADD(M_LO + ln * 4, 1u));
    if (c >= 0) *(LAS u16*)(L2_OUT + (c >> 3) * 1024 + ln * 16 + (c & 7) * 2) = (u16)e;
    else __builtin_amdgcn_raw_buffer_store_b32(e & 0x3FFFFFu, ro, LDS_ADD(M_OV, 1u) * 4, 0, AUX_SC1);
  }
  if (tid == 0) *(LAS v4u*)H_SYNC = (v4u){0u, 0u, 0u, 0u};
  WAITVM0();
  LBAR();
  STAMPG(1, 5);
  const unsigned L = *(LAS unsigned*)(M_OV + 8);
  if (8 * wave < L) store16(*(LAS v4u*)(L2_OUT + tid * 16), rh, tid * 16, ch * 8192, AUX_SC1);
  if (tid == 1023) __builtin_amdgcn_raw_buffer_store_b64(*(LAS v2u*)(M_OV), rh, H_META + ch * 128 + 64, 0, AUX_SC1);
}

#define GATHER_ASM \
  "s_waitcnt lgkmcnt(0)\n" \
  "v_lshlrev_b32_sdwa v8, 3, %4 dst_sel:DWORD dst_unused:UNUSED_PAD src0_sel:DWORD src1_sel:WORD_0\n" \
  "v_lshlrev_b32_sdwa v10, 3, %4 dst_sel:DWORD dst_unused:UNUSED_PAD src0_sel:DWORD src1_sel:WORD_1\n" \
  "ds_read_b64 v[8:9], v8\n" \
  "ds_read_b64 v[10:11], v10\n" \
  "v_lshlrev_b32_sdwa v12, 3, %5 dst_sel:DWORD dst_unused:UNUSED_PAD src0_sel:DWORD src1_sel:WORD_0\n" \
  "v_lshlrev_b32_sdwa v14, 3, %5 dst_sel:DWORD dst_unused:UNUSED_PAD src0_sel:DWORD src1_sel:WORD_1\n" \
  "ds_read_b64 v[12:13], v12\n" \
  "ds_read_b64 v[14:15], v14\n" \
  "v_lshlrev_b32_sdwa v16, 3, %6 dst_sel:DWORD dst_unused:UNUSED_PAD src0_sel:DWORD src1_sel:WORD_0\n" \
  "v_lshlrev_b32_sdwa v18, 3, %6 dst_sel:DWORD dst_unused:UNUSED_PAD src0_sel:DWORD src1_sel:WORD_1\n" \
  "ds_read_b64 v[16:17], v16\n" \
  "ds_read_b64 v[18:19], v18\n" \
  "v_lshlrev_b32_sdwa v20, 3, %7 dst_sel:DWORD dst_unused:UNUSED_PAD src0_sel:DWORD src1_sel:WORD_0\n" \
  "v_lshlrev_b32_sdwa v22, 3, %7 dst_sel:DWORD dst_unused:UNUSED_PAD src0_sel:DWORD src1_sel:WORD_1\n" \
  "ds_read_b64 v[20:21], v20\n" \
  "ds_read_b64 v[22:23], v22\n" \
  "s_waitcnt lgkmcnt(4)\n" \
  "v_smfmac_f32_16x16x64_bf16 %0, %1, v[8:15], %2\n" \
  "v_lshlrev_b32_sdwa v8, 3, %8 dst_sel:DWORD dst_unused:UNUSED_PAD src0_sel:DWORD src1_sel:WORD_0\n" \
  "v_lshlrev_b32_sdwa v10, 3, %8 dst_sel:DWORD dst_unused:UNUSED_PAD src0_sel:DWORD src1_sel:WORD_1\n" \
  "ds_read_b64 v[8:9], v8\n" \
  "ds_read_b64 v[10:11], v10\n" \
  "v_lshlrev_b32_sdwa v12, 3, %9 dst_sel:DWORD dst_unused:UNUSED_PAD src0_sel:DWORD src1_sel:WORD_0\n" \
  "v_lshlrev_b32_sdwa v14, 3, %9 dst_sel:DWORD dst_unused:UNUSED_PAD src0_sel:DWORD src1_sel:WORD_1\n" \
  "ds_read_b64 v[12:13], v12\n" \
  "ds_read_b64 v[14:15], v14\n" \
  "s_waitcnt lgkmcnt(4)\n" \
  "v_smfmac_f32_16x16x64_bf16 %0, %1, v[16:23], %2\n" \
  "v_lshlrev_b32_sdwa v16, 3, %10 dst_sel:DWORD dst_unused:UNUSED_PAD src0_sel:DWORD src1_sel:WORD_0\n" \
  "v_lshlrev_b32_sdwa v18, 3, %10 dst_sel:DWORD dst_unused:UNUSED_PAD src0_sel:DWORD src1_sel:WORD_1\n" \
  "ds_read_b64 v[16:17], v16\n" \
  "ds_read_b64 v[18:19], v18\n" \
  "v_lshlrev_b32_sdwa v20, 3, %11 dst_sel:DWORD dst_unused:UNUSED_PAD src0_sel:DWORD src1_sel:WORD_0\n" \
  "v_lshlrev_b32_sdwa v22, 3, %11 dst_sel:DWORD dst_unused:UNUSED_PAD src0_sel:DWORD src1_sel:WORD_1\n" \
  "ds_read_b64 v[20:21], v20\n" \
  "ds_read_b64 v[22:23], v22\n" \
  "s_waitcnt lgkmcnt(4)\n" \
  "v_smfmac_f32_16x16x64_bf16 %0, %1, v[8:15], %2\n" \
  "v_lshlrev_b32_sdwa v8, 3, %12 dst_sel:DWORD dst_unused:UNUSED_PAD src0_sel:DWORD src1_sel:WORD_0\n" \
  "v_lshlrev_b32_sdwa v10, 3, %12 dst_sel:DWORD dst_unused:UNUSED_PAD src0_sel:DWORD src1_sel:WORD_1\n" \
  "ds_read_b64 v[8:9], v8\n" \
  "ds_read_b64 v[10:11], v10\n" \
  "v_lshlrev_b32_sdwa v12, 3, %13 dst_sel:DWORD dst_unused:UNUSED_PAD src0_sel:DWORD src1_sel:WORD_0\n" \
  "v_lshlrev_b32_sdwa v14, 3, %13 dst_sel:DWORD dst_unused:UNUSED_PAD src0_sel:DWORD src1_sel:WORD_1\n" \
  "ds_read_b64 v[12:13], v12\n" \
  "ds_read_b64 v[14:15], v14\n" \
  "s_waitcnt lgkmcnt(4)\n" \
  "v_smfmac_f32_16x16x64_bf16 %0, %1, v[16:23], %2\n" \
  "v_lshlrev_b32_sdwa v16, 3, %14 dst_sel:DWORD dst_unused:UNUSED_PAD src0_sel:DWORD src1_sel:WORD_0\n" \
  "v_lshlrev_b32_sdwa v18, 3, %14 dst_sel:DWORD dst_unused:UNUSED_PAD src0_sel:DWORD src1_sel:WORD_1\n" \
  "ds_read_b64 v[16:17], v16\n" \
  "ds_read_b64 v[18:19], v18\n" \
  "v_lshlrev_b32_sdwa v20, 3, %15 dst_sel:DWORD dst_unused:UNUSED_PAD src0_sel:DWORD src1_sel:WORD_0\n" \
  "v_lshlrev_b32_sdwa v22, 3, %15 dst_sel:DWORD dst_unused:UNUSED_PAD src0_sel:DWORD src1_sel:WORD_1\n" \
  "ds_read_b64 v[20:21], v20\n" \
  "ds_read_b64 v[22:23], v22\n" \
  "s_waitcnt lgkmcnt(4)\n" \
  "v_smfmac_f32_16x16x64_bf16 %0, %1, v[8:15], %2\n" \
  "v_lshlrev_b32_sdwa v8, 3, %16 dst_sel:DWORD dst_unused:UNUSED_PAD src0_sel:DWORD src1_sel:WORD_0\n" \
  "v_lshlrev_b32_sdwa v10, 3, %16 dst_sel:DWORD dst_unused:UNUSED_PAD src0_sel:DWORD src1_sel:WORD_1\n" \
  "ds_read_b64 v[8:9], v8\n" \
  "ds_read_b64 v[10:11], v10\n" \
  "v_lshlrev_b32_sdwa v12, 3, %17 dst_sel:DWORD dst_unused:UNUSED_PAD src0_sel:DWORD src1_sel:WORD_0\n" \
  "v_lshlrev_b32_sdwa v14, 3, %17 dst_sel:DWORD dst_unused:UNUSED_PAD src0_sel:DWORD src1_sel:WORD_1\n" \
  "ds_read_b64 v[12:13], v12\n" \
  "ds_read_b64 v[14:15], v14\n" \
  "s_waitcnt lgkmcnt(4)\n" \
  "v_smfmac_f32_16x16x64_bf16 %0, %1, v[16:23], %2\n" \
  "v_lshlrev_b32_sdwa v16, 3, %18 dst_sel:DWORD dst_unused:UNUSED_PAD src0_sel:DWORD src1_sel:WORD_0\n" \
  "v_lshlrev_b32_sdwa v18, 3, %18 dst_sel:DWORD dst_unused:UNUSED_PAD src0_sel:DWORD src1_sel:WORD_1\n" \
  "ds_read_b64 v[16:17], v16\n" \
  "ds_read_b64 v[18:19], v18\n" \
  "v_lshlrev_b32_sdwa v20, 3, %19 dst_sel:DWORD dst_unused:UNUSED_PAD src0_sel:DWORD src1_sel:WORD_0\n" \
  "v_lshlrev_b32_sdwa v22, 3, %19 dst_sel:DWORD dst_unused:UNUSED_PAD src0_sel:DWORD src1_sel:WORD_1\n" \
  "ds_read_b64 v[20:21], v20\n" \
  "ds_read_b64 v[22:23], v22\n" \
  "s_cmp_le_u32 %3, 16\n" \
  "s_cbranch_scc1 Lgt%=\n" \
  "s_waitcnt lgkmcnt(4)\n" \
  "v_smfmac_f32_16x16x64_bf16 %0, %1, v[8:15], %2\n" \
  "v_lshlrev_b32_sdwa v8, 3, %20 dst_sel:DWORD dst_unused:UNUSED_PAD src0_sel:DWORD src1_sel:WORD_0\n" \
  "v_lshlrev_b32_sdwa v10, 3, %20 dst_sel:DWORD dst_unused:UNUSED_PAD src0_sel:DWORD src1_sel:WORD_1\n" \
  "ds_read_b64 v[8:9], v8\n" \
  "ds_read_b64 v[10:11], v10\n" \
  "v_lshlrev_b32_sdwa v12, 3, %21 dst_sel:DWORD dst_unused:UNUSED_PAD src0_sel:DWORD src1_sel:WORD_0\n" \
  "v_lshlrev_b32_sdwa v14, 3, %21 dst_sel:DWORD dst_unused:UNUSED_PAD src0_sel:DWORD src1_sel:WORD_1\n" \
  "ds_read_b64 v[12:13], v12\n" \
  "ds_read_b64 v[14:15], v14\n" \
  "s_cmp_le_u32 %3, 18\n" \
  "s_cbranch_scc1 Lgt%=\n" \
  "s_waitcnt lgkmcnt(4)\n" \
  "v_smfmac_f32_16x16x64_bf16 %0, %1, v[16:23], %2\n" \
  "v_lshlrev_b32_sdwa v16, 3, %22 dst_sel:DWORD dst_unused:UNUSED_PAD src0_sel:DWORD src1_sel:WORD_0\n" \
  "v_lshlrev_b32_sdwa v18, 3, %22 dst_sel:DWORD dst_unused:UNUSED_PAD src0_sel:DWORD src1_sel:WORD_1\n" \
  "ds_read_b64 v[16:17], v16\n" \
  "ds_read_b64 v[18:19], v18\n" \
  "v_lshlrev_b32_sdwa v20, 3, %23 dst_sel:DWORD dst_unused:UNUSED_PAD src0_sel:DWORD src1_sel:WORD_0\n" \
  "v_lshlrev_b32_sdwa v22, 3, %23 dst_sel:DWORD dst_unused:UNUSED_PAD src0_sel:DWORD src1_sel:WORD_1\n" \
  "ds_read_b64 v[20:21], v20\n" \
  "ds_read_b64 v[22:23], v22\n" \
  "s_cmp_le_u32 %3, 20\n" \
  "s_cbranch_scc1 Lgt%=\n" \
  "s_waitcnt lgkmcnt(4)\n" \
  "v_smfmac_f32_16x16x64_bf16 %0, %1, v[8:15], %2\n" \
  "v_lshlrev_b32_sdwa v8, 3, %24 dst_sel:DWORD dst_unused:UNUSED_PAD src0_sel:DWORD src1_sel:WORD_0\n" \
  "v_lshlrev_b32_sdwa v10, 3, %24 dst_sel:DWORD dst_unused:UNUSED_PAD src0_sel:DWORD src1_sel:WORD_1\n" \
  "ds_read_b64 v[8:9], v8\n" \
  "ds_read_b64 v[10:11], v10\n" \
  "v_lshlrev_b32_sdwa v12, 3, %25 dst_sel:DWORD dst_unused:UNUSED_PAD src0_sel:DWORD src1_sel:WORD_0\n" \
  "v_lshlrev_b32_sdwa v14, 3, %25 dst_sel:DWORD dst_unused:UNUSED_PAD src0_sel:DWORD src1_sel:WORD_1\n" \
  "ds_read_b64 v[12:13], v12\n" \
  "ds_read_b64 v[14:15], v14\n" \
  "s_cmp_le_u32 %3, 22\n" \
  "s_cbranch_scc1 Lgt%=\n" \
  "s_waitcnt lgkmcnt(4)\n" \
  "v_smfmac_f32_16x16x64_bf16 %0, %1, v[16:23], %2\n" \
  "v_lshlrev_b32_sdwa v16, 3, %26 dst_sel:DWORD dst_unused:UNUSED_PAD src0_sel:DWORD src1_sel:WORD_0\n" \
  "v_lshlrev_b32_sdwa v18, 3, %26 dst_sel:DWORD dst_unused:UNUSED_PAD src0_sel:DWORD src1_sel:WORD_1\n" \
  "ds_read_b64 v[16:17], v16\n" \
  "ds_read_b64 v[18:19], v18\n" \
  "v_lshlrev_b32_sdwa v20, 3, %27 dst_sel:DWORD dst_unused:UNUSED_PAD src0_sel:DWORD src1_sel:WORD_0\n" \
  "v_lshlrev_b32_sdwa v22, 3, %27 dst_sel:DWORD dst_unused:UNUSED_PAD src0_sel:DWORD src1_sel:WORD_1\n" \
  "ds_read_b64 v[20:21], v20\n" \
  "ds_read_b64 v[22:23], v22\n" \
  "s_cmp_le_u32 %3, 24\n" \
  "s_cbranch_scc1 Lgt%=\n" \
  "s_waitcnt lgkmcnt(4)\n" \
  "v_smfmac_f32_16x16x64_bf16 %0, %1, v[8:15], %2\n" \
  "v_lshlrev_b32_sdwa v8, 3, %28 dst_sel:DWORD dst_unused:UNUSED_PAD src0_sel:DWORD src1_sel:WORD_0\n" \
  "v_lshlrev_b32_sdwa v10, 3, %28 dst_sel:DWORD dst_unused:UNUSED_PAD src0_sel:DWORD src1_sel:WORD_1\n" \
  "ds_read_b64 v[8:9], v8\n" \
  "ds_read_b64 v[10:11], v10\n" \
  "v_lshlrev_b32_sdwa v12, 3, %29 dst_sel:DWORD dst_unused:UNUSED_PAD src0_sel:DWORD src1_sel:WORD_0\n" \
  "v_lshlrev_b32_sdwa v14, 3, %29 dst_sel:DWORD dst_unused:UNUSED_PAD src0_sel:DWORD src1_sel:WORD_1\n" \
  "ds_read_b64 v[12:13], v12\n" \
  "ds_read_b64 v[14:15], v14\n" \
  "s_cmp_le_u32 %3, 26\n" \
  "s_cbranch_scc1 Lgt%=\n" \
  "s_waitcnt lgkmcnt(4)\n" \
  "v_smfmac_f32_16x16x64_bf16 %0, %1, v[16:23], %2\n" \
  "v_lshlrev_b32_sdwa v16, 3, %30 dst_sel:DWORD dst_unused:UNUSED_PAD src0_sel:DWORD src1_sel:WORD_0\n" \
  "v_lshlrev_b32_sdwa v18, 3, %30 dst_sel:DWORD dst_unused:UNUSED_PAD src0_sel:DWORD src1_sel:WORD_1\n" \
  "ds_read_b64 v[16:17], v16\n" \
  "ds_read_b64 v[18:19], v18\n" \
  "v_lshlrev_b32_sdwa v20, 3, %31 dst_sel:DWORD dst_unused:UNUSED_PAD src0_sel:DWORD src1_sel:WORD_0\n" \
  "v_lshlrev_b32_sdwa v22, 3, %31 dst_sel:DWORD dst_unused:UNUSED_PAD src0_sel:DWORD src1_sel:WORD_1\n" \
  "ds_read_b64 v[20:21], v20\n" \
  "ds_read_b64 v[22:23], v22\n" \
  "s_cmp_le_u32 %3, 28\n" \
  "s_cbranch_scc1 Lgt%=\n" \
  "s_waitcnt lgkmcnt(4)\n" \
  "v_smfmac_f32_16x16x64_bf16 %0, %1, v[8:15], %2\n" \
  "v_lshlrev_b32_sdwa v8, 3, %32 dst_sel:DWORD dst_unused:UNUSED_PAD src0_sel:DWORD src1_sel:WORD_0\n" \
  "v_lshlrev_b32_sdwa v10, 3, %32 dst_sel:DWORD dst_unused:UNUSED_PAD src0_sel:DWORD src1_sel:WORD_1\n" \
  "ds_read_b64 v[8:9], v8\n" \
  "ds_read_b64 v[10:11], v10\n" \
  "v_lshlrev_b32_sdwa v12, 3, %33 dst_sel:DWORD dst_unused:UNUSED_PAD src0_sel:DWORD src1_sel:WORD_0\n" \
  "v_lshlrev_b32_sdwa v14, 3, %33 dst_sel:DWORD dst_unused:UNUSED_PAD src0_sel:DWORD src1_sel:WORD_1\n" \
  "ds_read_b64 v[12:13], v12\n" \
  "ds_read_b64 v[14:15], v14\n" \
  "s_cmp_le_u32 %3, 30\n" \
  "s_cbranch_scc1 Lgt%=\n" \
  "s_waitcnt lgkmcnt(4)\n" \
  "v_smfmac_f32_16x16x64_bf16 %0, %1, v[16:23], %2\n" \
  "v_lshlrev_b32_sdwa v16, 3, %34 dst_sel:DWORD dst_unused:UNUSED_PAD src0_sel:DWORD src1_sel:WORD_0\n" \
  "v_lshlrev_b32_sdwa v18, 3, %34 dst_sel:DWORD dst_unused:UNUSED_PAD src0_sel:DWORD src1_sel:WORD_1\n" \
  "ds_read_b64 v[16:17], v16\n" \
  "ds_read_b64 v[18:19], v18\n" \
  "v_lshlrev_b32_sdwa v20, 3, %35 dst_sel:DWORD dst_unused:UNUSED_PAD src0_sel:DWORD src1_sel:WORD_0\n" \
  "v_lshlrev_b32_sdwa v22, 3, %35 dst_sel:DWORD dst_unused:UNUSED_PAD src0_sel:DWORD src1_sel:WORD_1\n" \
  "ds_read_b64 v[20:21], v20\n" \
  "ds_read_b64 v[22:23], v22\n" \
  "Lgt%=:\n" \
  "s_waitcnt lgkmcnt(0)\n" \
  "v_smfmac_f32_16x16x64_bf16 %0, %1, v[8:15], %2\n" \
  "v_smfmac_f32_16x16x64_bf16 %0, %1, v[16:23], %2\n" \
  "s_nop 7\n" \
  ""
#define GATHER_CLOB "v8", "v9", "v10", "v11", "v12", "v13", "v14", "v15", "v16", "v17", "v18", "v19", "v20", "v21", "v22", "v23", "scc"

DEVI v4f gather(const v4u (&L)[8], int n, v8bf sel, int sidx) {
  v4f acc = {0.f, 0.f, 0.f, 0.f};
  asm volatile(GATHER_ASM : "+v"(acc) : "v"(sel), "v"(sidx), "s"(n),
    "v"(L[0][0]), "v"(L[0][1]), "v"(L[0][2]), "v"(L[0][3]), "v"(L[1][0]), "v"(L[1][1]), "v"(L[1][2]), "v"(L[1][3]),
    "v"(L[2][0]), "v"(L[2][1]), "v"(L[2][2]), "v"(L[2][3]), "v"(L[3][0]), "v"(L[3][1]), "v"(L[3][2]), "v"(L[3][3]),
    "v"(L[4][0]), "v"(L[4][1]), "v"(L[4][2]), "v"(L[4][3]), "v"(L[5][0]), "v"(L[5][1]), "v"(L[5][2]), "v"(L[5][3]),
    "v"(L[6][0]), "v"(L[6][1]), "v"(L[6][2]), "v"(L[6][3]), "v"(L[7][0]), "v"(L[7][1]), "v"(L[7][2]), "v"(L[7][3])
    : GATHER_CLOB, "memory");
  return acc;
}
DEVI v4f slow_edges(v4f acc, const char* ws, int ch, int n2, int any1, int lane) {
  const __amdgpu_buffer_rsrc_t ro = mkrsrc(ws + WS_O2 + (size_t)ch * SEGB, SEGB);
  const int lists = any1 ? G1 : 0;
  for (int li = -1; li < lists; ++li) {
    const int cnt = li < 0 ? n2 : (int)((const unsigned*)(ws + WS_O1C))[li];
    for (int base = 0; base < cnt; base += 64) {
      bool ok = base + lane < cnt;
      unsigned tl = 0, p = 0;
      if (ok) {
        if (li < 0) { const unsigned e = __builtin_amdgcn_raw_buffer_load_b32(ro, (base + lane) * 4, 0, AUX_SC1); tl = e >> 16; p = e & 0xFFFF; }
        else { const unsigned e = ((const unsigned*)(ws + WS_O1))[li * 2048 + base + lane]; ok = (int)(e >> 20) == ch; tl = (e >> 14) & 63; p = e & 0x3FFF; }
      }
      const v2u val = *(LAS v2u*)(p * 8);
      u64 m = __builtin_amdgcn_ballot_w64(ok);
      while (m) {
        const int i = __builtin_ctzll(m); m &= m - 1;
        const unsigned t = __builtin_amdgcn_readlane(tl, i), a = __builtin_amdgcn_readlane(val[0], i), c = __builtin_amdgcn_readlane(val[1], i);
        if ((unsigned)lane == t) { acc[0] += bflo(a); acc[1] += bfhi(a); acc[2] += bflo(c); acc[3] += bfhi(c); }
      }
    }
  }
  return acc;
}
DEVI unsigned pksafe(float a, float b) { return umin(pkbf(a, b), 0xFFFFFFFEu); }
DEVI v2u publish(v4f a, float d2, __amdgpu_buffer_rsrc_t rs, int soff, int lane) {
  const unsigned x0 = pksafe(a[0] * d2, a[1] * d2), x1 = pksafe(a[2] * d2, a[3] * d2);
  const unsigned y0 = dppu<0xF5>(x0), y1 = dppu<0xF5>(x1);
  if (!(lane & 1)) store16(((v4u){x0, x1, y0, y1}), rs, lane * 8, soff, AUX_SC1);
  return (v2u){x0, x1};
}
DEVI void issue_lists(v4u (&L)[8], v2u& ov, const __amdgpu_buffer_rsrc_t rh, const int c, const int n, const int l16) {
  CFENCE();
#pragma unroll
  for (int i = 0; i < 8; ++i) L[i] = load16(rh, l16, 4 * i < n ? c * 8192 + i * 1024 : OOB, AUX_SC1);
  ov = __builtin_bit_cast(v2u, __builtin_amdgcn_raw_buffer_load_b64(rh, H_META + 64, c * 128, AUX_SC1));
}
DEVI void check_lists(v4u (&L)[8], v2u& ov, const char* ws, const int c, const int n, const int l16) {
  const char* pa = ws + WS_LIST + c * 8192 + 4096;
  const char* pm = ws + WS_META + c * 128;
  unsigned sp = 0;
  bool redo;
#pragma unroll 1
  do {
    asm volatile("s_cmp_eq_u32 %12, 0\n\ts_cbranch_scc1 Lck%=\n\ts_sleep 8\n\t"
                 "global_load_dwordx4 %0, %9, %10 offset:-4096 sc1\n\tglobal_load_dwordx4 %1, %9, %10 offset:-3072 sc1\n\tglobal_load_dwordx4 %2, %9, %10 offset:-2048 sc1\n\t"
                 "global_load_dwordx4 %3, %9, %10 offset:-1024 sc1\n\tglobal_load_dwordx4 %4, %9, %10 sc1\n\tglobal_load_dwordx4 %5, %9, %10 offset:1024 sc1\n\t"
                 "global_load_dwordx4 %6, %9, %10 offset:2048 sc1\n\tglobal_load_dwordx4 %7, %9, %10 offset:3072 sc1\n\tglobal_load_dwordx2 %8, %13, %11 sc1\n\ts_waitcnt vmcnt(0)\n"
                 "Lck%=:"
                 : "+v"(L[0]), "+v"(L[1]), "+v"(L[2]), "+v"(L[3]), "+v"(L[4]), "+v"(L[5]), "+v"(L[6]), "+v"(L[7]), "+v"(ov) : "v"(l16), "s"(pa), "s"(pm), "s"(sp), "v"(64) : "memory", "scc");
    unsigned m = umax(ov[0], ov[1]);
#pragma unroll
    for (int i = 0; i < 8; ++i) m = umax(m, (i < 4 || 4 * i < n) ? max4(L[i]) : 0u);
    redo = anyl(m == ~0u) & (++sp < SPINCAP);
  } while (redo);
}
DEVI unsigned wait_flags(const __amdgpu_buffer_rsrc_t rh, const int voff) {
  unsigned f;
#pragma unroll 1
  for (unsigned sp = 0; sp < SPINCAP; ++sp) {
    CFENCE();
    f = __builtin_amdgcn_raw_buffer_load_b32(rh, voff, H_FLAG, AUX_SC1);
    if (!anyl(f == ~0u)) break;
    __builtin_amdgcn_s_sleep(8);
  }
  CFENCE();
  return f;
}
#define TC1 12
#define TC2 36
#define TCL 210
#define TC0 10
#define XSLEEP 6

template <int I, int E, class F> DEVI void sfor(F&& f) { if constexpr (I < E) { f(IC<I>{}); sfor<I + 1, E>(f); } }
template <int RM, int BM> DEVI float dppmerge(float old, float x) { return u2f((unsigned)__builtin_amdgcn_update_dpp((int)f2u(old), (int)f2u(x), 0xE4, RM, BM, false)); }
DEVI int lane_again() { int l; asm volatile("v_mbcnt_lo_u32_b32 %0, -1, 0\n\tv_mbcnt_hi_u32_b32 %0, -1, %0" : "=v"(l)); return l; }
DEVI float bperm(int byteaddr, float v) { return u2f((unsigned)__builtin_amdgcn_ds_bpermute(byteaddr, (int)f2u(v))); }
DEVI float vmax(float a, float b) { float r; asm("v_max_f32 %0, %1, %2" : "=v"(r) : "v"(a), "v"(b)); return r; }
DEVI float vmax32(float x) { v2u r = __builtin_amdgcn_permlane32_swap(f2u(x), f2u(x), false, false); return vmax(u2f(r[0]), u2f(r[1])); }
DEVI float swapadd16(float a, float b) { v2u r = __builtin_amdgcn_permlane16_swap(f2u(a), f2u(b), false, false); return u2f(r[0]) + u2f(r[1]); }
DEVI float swapadd32(float a, float b) { v2u r = __builtin_amdgcn_permlane32_swap(f2u(a), f2u(b), false, false); return u2f(r[0]) + u2f(r[1]); }
DEVI v8bf pack8(const v16f& a, int o) { return __builtin_bit_cast(v8bf, (v4u){pkbf(a[o], a[o + 1]), pkbf(a[o + 2], a[o + 3]), pkbf(a[o + 4], a[o + 5]), pkbf(a[o + 6], a[o + 7])}); }
#define TSTAMP(i) do {} while (0)
#define MFMA16(a, b, c) __builtin_amdgcn_mfma_f32_16x16x32_bf16(a, b, c, 0, 0, 0)
#define MFMA32(a, b, c) __builtin_amdgcn_mfma_f32_32x32x16_bf16(a, b, c, 0, 0, 0)
#define PIN() __builtin_amdgcn_sched_barrier(0x406)
DEVI void tf_batch(const int wave, const int nmask, const v8bf (&mreg)[RF],  const float cc, const unsigned nd01, const unsigned nd23, const char* ws,
                   u16* __restrict__ out, const int lane) {
  constexpr int PD = 3;
  const int h = lane >> 5, n = (lane >> 3) & 3, hop = lane & 7;
  const v16f zero = {0.f, 0.f, 0.f, 0.f, 0.f, 0.f, 0.f, 0.f, 0.f, 0.f, 0.f, 0.f, 0.f, 0.f, 0.f, 0.f};
  LAS char* const wb = ldsbase(T_W + lane * 16);
  LAS char* const scw = ldsbase(T_SCR + wave * 256 + lane * 4);
  LAS char* const scr = ldsbase(T_SCR + wave * 256 + n * 32 + h * 16);
  __builtin_amdgcn_s_setprio(1);
  v8bf go[8];
  {
    LAS char* const a0 = ldsbase(T_TOK + (hop ^ 4) * T_HS + h * 800 + wave * 32 + (n & nmask) * 8);
    LAS char* const a1 = ldsbase(T_TOK + (hop ^ 4) * T_HS + h * 800 + wave * 32 + (n & nmask) * 8 + 400);
#pragma unroll
    for (int s = 0; s < 8; ++s) {
      const v4bf lo = *(volatile LAS v4bf*)(a0 + s * 1600), hi = *(volatile LAS v4bf*)(a1 + s * 1600);
      go[s] = __builtin_shufflevector(lo, hi, 0, 1, 2, 3, 4, 5, 6, 7);
    }
  }
  v8bf xreg[8];
  LAS char* xb;
  {
    const int row = lane & 15, r = row & 3, up = row >> 3;
    xb = ldsbase((((row >> 2) ^ (lane >> 4)) & 1) || (r == 2 && up) ? 0x7F000 : T_X + (r + (r == 3 ? up : 0)) * 256 + h * 16);
#pragma unroll
    for (int s = 0; s < 3; ++s) xreg[s] = *(LAS v8bf*)(xb + s * 32);
  }
  const float wmean = u2f(__builtin_amdgcn_readfirstlane(*(LAS unsigned*)(T_X + 1280)));
  PIN();
  TSTAMP(0);
  v4f xq = {0.f, 0.f, 0.f, 0.f};
  v4f gr0 = xq, gr1 = xq;
#pragma unroll
  for (int s = 0; s < 8; ++s) {
    const v4s lo = __builtin_bit_cast(v4s, __builtin_shufflevector(go[s], go[s], 0, 1, 2, 3)), hi = __builtin_bit_cast(v4s, __builtin_shufflevector(go[s], go[s], 4, 5, 6, 7));
    gr0 = __builtin_amdgcn_mfma_f32_4x4x4bf16_1k(lo, lo, gr0, 0, 0, 0);
    gr1 = __builtin_amdgcn_mfma_f32_4x4x4bf16_1k(hi, hi, gr1, 0, 0, 0);
    if (s + 3 < 8) xreg[s + 3] = *(LAS v8bf*)(xb + (s + 3) * 32);
    xq = MFMA16(xreg[s], go[s], xq);
    __builtin_amdgcn_sched_barrier(0);
  }
  PIN();
  TSTAMP(1);
  v8bf w[64], zb[4][2], pa[2];
  v16f acc[8], S;
  v4f rl, ga, gb, sc;
  float rho, t1, t3;
  unsigned p0, p1;
#pragma unroll
  for (int f = 0; f < RF; ++f) w[f] = mreg[f];
  sfor<0, 64>([&](auto fi) __attribute__((always_inline)) NOPK {
    constexpr int f = decltype(fi)::v;
    if constexpr (f + PD >= RF && f + PD < 64) w[f + PD] = *(LAS v8bf*)(wb + (f + PD - RF) * 1024);
    constexpr int T = f >> 3, s = f & 7;
    acc[T] = T < 4 ? MFMA32(w[f], go[s], s == 0 ? zero : acc[T]) : MFMA32(go[s], w[f], s == 0 ? zero : acc[T]);
    if constexpr (T > 0 && (s == 1 || s == 2)) {
      constexpr int U = T - 1, sg = s - 1;
      if constexpr (U < 4) S = MFMA32(go[2 * U + sg], pack8(acc[U], 8 * sg), (U == 0 && sg == 0) ? zero : S);
      else { zb[U - 4][sg] = pack8(acc[U], 8 * sg); asm volatile("" : "+v"(zb[U - 4][sg])); }
    }
    if constexpr (f == 3) {
      float e0 = gr0[0] + gr1[0], e1 = gr0[1] + gr1[1], e2 = gr0[2] + gr1[2], e3 = gr0[3] + gr1[3];
      asm("" : "+v"(e0), "+v"(e1), "+v"(e2), "+v"(e3));
      const float s2 = (lane & 2) ? ((lane & 1) ? e3 : e2) : ((lane & 1) ? e1 : e0);
      const float var = vmax(__builtin_fmaf(xq[0] * (-1.f / 128.f), xq[0], add32(s2)), 0.f) * (cc * cc * (1.f / 128.f));
      rho = cc * __builtin_amdgcn_rsqf(var + 1e-5f);
    }
    if constexpr (f == 30) { *(LAS float*)scw = h ? rho * xq[0] * (-1.f / 128.f) : rho; rl = *(LAS v4f*)scr; ga = *(LAS v4f*)(scr + 128); }
    if constexpr (f == 37) {
#pragma unroll
      for (int r = 0; r < 4; ++r) sc[r] = __builtin_fmaf(dppmerge<0xA, 0xC>(dppmerge<0xA, 0x3>(dppmerge<0x5, 0xC>(S[r], S[4 + r]), S[8 + r]), S[12 + r]), rl[r], xq[1] * ga[r]);
      asm volatile("" : "+v"(sc));
      *(LAS float*)scw = xq[3]; ga = *(LAS v4f*)scr; gb = *(LAS v4f*)(scr + 128);
    }
    if constexpr (f == 46) {
      const float m = vmax32(vmax(vmax(sc[0], sc[1]), vmax(sc[2], sc[3]))) * rho;
#pragma unroll
      for (int r = 0; r < 4; ++r) sc[r] = __builtin_amdgcn_exp2f(__builtin_fmaf(sc[r], rho, -m));
      const float inv = __builtin_amdgcn_rcpf(add32((sc[0] + sc[1]) + (sc[2] + sc[3])) * cc);
#pragma unroll
      for (int r = 0; r < 4; ++r) sc[r] *= rl[r] * inv;
      p0 = pkbf(sc[0], sc[1]); p1 = pkbf(sc[2], sc[3]);
      t1 = xq[0] + add32(__builtin_fmaf(sc[3], ga[3], __builtin_fmaf(sc[2], ga[2], __builtin_fmaf(sc[1], ga[1], sc[0] * ga[0]))));
      t3 = add32(__builtin_fmaf(sc[3], gb[3], __builtin_fmaf(sc[2], gb[2], __builtin_fmaf(sc[1], gb[1], __builtin_fmaf(sc[0], gb[0], xq[2])))));
      t3 = __builtin_fmaf(-t1, wmean, t3);
      asm volatile("" : "+v"(p0), "+v"(p1), "+v"(t1), "+v"(t3));
    }
    if constexpr (f == 15 || f == 31 || f == 47 || f == 63) TSTAMP(2 + (f >> 4));
    PIN();
  });
  zb[3][0] = pack8(acc[7], 0); zb[3][1] = pack8(acc[7], 8);
  const int l2 = lane_again(), h2 = l2 >> 5, n2 = (l2 >> 3) & 3, hop2 = l2 & 7;
  const unsigned one = (hop2 >> 2) == h2 ? ((hop2 & 1) ? 0x3F800000u : 0x3F80u) : 0u;
  const unsigned d0 = (hop2 & 2) ? 0u : one, d1 = (hop2 & 2) ? one : 0u;
  v8bf da[2];
  pa[0] = __builtin_bit_cast(v8bf, (v4u){n2 == 0 ? p0 : 0u, n2 == 0 ? p1 : 0u, n2 == 1 ? p0 : 0u, n2 == 1 ? p1 : 0u});
  pa[1] = __builtin_bit_cast(v8bf, (v4u){n2 == 2 ? p0 : 0u, n2 == 2 ? p1 : 0u, n2 == 3 ? p0 : 0u, n2 == 3 ? p1 : 0u});
  da[0] = __builtin_bit_cast(v8bf, (v4u){n2 == 0 ? d0 : 0u, n2 == 0 ? d1 : 0u, n2 == 1 ? d0 : 0u, n2 == 1 ? d1 : 0u});
  da[1] = __builtin_bit_cast(v8bf, (v4u){n2 == 2 ? d0 : 0u, n2 == 2 ? d1 : 0u, n2 == 3 ? d0 : 0u, n2 == 3 ? d1 : 0u});
  LAS char* const tr0 = ldsbase(T_TOK + (4 - 4 * h2 + ((l2 >> 2) & 3)) * T_HS + (((l2 >> 4) & 1) * 4 + (l2 & 3)) * 400 + wave * 32);
  LAS char* const tr1 = ldsbase(T_TOK + (4 - 4 * h2 + ((l2 >> 2) & 3)) * T_HS + (((l2 >> 4) & 1) * 4 + (l2 & 3)) * 400 + wave * 32 + (2 & nmask) * 8);
  v8bf gt[4][2];
  auto rdgt = [&](int T) __attribute__((always_inline)) NOPK {
#pragma unroll
    for (int sg = 0; sg < 2; ++sg) {
      LAS char* const q = (sg ? tr1 : tr0) + T * 3200;
      gt[T][sg] = __builtin_shufflevector(__builtin_amdgcn_ds_read_tr16_b64_v4bf16((LAS v4bf*)q), __builtin_amdgcn_ds_read_tr16_b64_v4bf16((LAS v4bf*)(q + 8)), 0, 1, 2, 3, 4, 5, 6, 7);
    }
  };
  rdgt(0);
  PIN();
  v16f O[4], q;
#pragma unroll
  for (int T = 0; T < 4; ++T) {
    if (T < 3) rdgt(T + 1);
    PIN();
    O[T] = MFMA32(pa[0], zb[T][0], zero);
    O[T] = MFMA32(pa[1], zb[T][1], O[T]);
    O[T] = MFMA32(da[0], gt[T][0], O[T]);
    O[T] = MFMA32(da[1], gt[T][1], O[T]);
    if (T == 1) q = O[0] * O[0];
    if (T > 1) q += O[T - 1] * O[T - 1];
    if (T > 0) asm volatile("" : "+v"(q));
    PIN();
  }
  __builtin_amdgcn_s_setprio(0);
  TSTAMP(6);
  q += O[3] * O[3];
  float q8[8], q4[4], q2[2];
#pragma unroll
  for (int i = 0; i < 8; ++i) q8[i] = swapadd16(q[i], q[8 + i]);
  const bool b8 = l2 & 8, b4 = l2 & 4, b2 = l2 & 2;
#pragma unroll
  for (int i = 0; i < 4; ++i) q4[i] = (b8 ? q8[4 + i] : q8[i]) + dppf<0x128>(b8 ? q8[i] : q8[4 + i]);
#pragma unroll
  for (int i = 0; i < 2; ++i) q2[i] = (b4 ? q4[2 + i] : q4[i]) + dppf<DPP_HM>(b4 ? q4[i] : q4[2 + i]);
  float qs = (b2 ? q2[1] : q2[0]) + dppf<DPP_X2>(b2 ? q2[0] : q2[1]);
  qs += dppf<DPP_X1>(qs);
  const int l3 = lane_again();
  qs = bperm(32 * (l3 & 4) + 4 * (l3 & 24) + 8 * (l3 & 3), qs);
  TSTAMP(7);
  const float var = vmax(__builtin_fmaf(t1 * (-1.f / 128.f), t1, qs), 0.f) * cc * (cc * (1.f / 128.f));
  const float a = t3 * (cc * 1.4426950408889634f) * __builtin_amdgcn_rsqf(var + 1e-5f);
  float m = vmax(a, dppf<DPP_X1>(a)); m = vmax(m, dppf<DPP_X2>(m)); m = vmax(m, dppf<DPP_HM>(m));
  const float e = __builtin_amdgcn_exp2f(a - m);
  float sm = e + dppf<DPP_X1>(e); sm += dppf<DPP_X2>(sm); sm += dppf<DPP_HM>(sm);
  *(LAS float*)(T_SCR + wave * 256 + l3 * 4) = e * cc * __builtin_amdgcn_rcpf(sm);
  LAS char* const gr = ldsbase(T_SCR + wave * 256 + (l3 >> 5) * 16);
  v4f gam[4];
#pragma unroll
  for (int k = 0; k < 4; ++k) gam[k] = *(LAS v4f*)(gr + 32 * k);
  float y[2][4];
#pragma unroll
  for (int T = 0; T < 4; ++T) {
    float v[4];
#pragma unroll
    for (int k = 0; k < 4; ++k) v[k] = __builtin_fmaf(gam[k][3], O[T][4 * k + 3], __builtin_fmaf(gam[k][2], O[T][4 * k + 2], __builtin_fmaf(gam[k][1], O[T][4 * k + 1], gam[k][0] * O[T][4 * k])));
    y[0][T] = swapadd32(v[0], v[1]);
    y[1][T] = swapadd32(v[2], v[3]);
  }
  TSTAMP(8);
  const int ln = lane_again();
  const unsigned node[2] = {ln < 32 ? nd01 & 0xFFFFu : nd01 >> 16, ln < 32 ? nd23 & 0xFFFFu : nd23 >> 16};
#pragma unroll
  for (int k = 0; k < 2; ++k)
    if (node[k] < (unsigned)NN) {
      u16* o = out + node[k] * 128 + (ln & 31);
      const unsigned p01 = pkbf(y[k][0], y[k][1]), p23 = pkbf(y[k][2], y[k][3]);
      o[0] = (u16)p01; o[32] = (u16)(p01 >> 16); o[64] = (u16)p23; o[96] = (u16)(p23 >> 16);
    }
}
__global__ void NOPK __launch_bounds__(1024, 4) k_main(char* ws, const u16* __restrict__ wr, u16* __restrict__ out) {
  const int b = BID, slice = (b & 7) * 4 + (b >> 6), part = (b >> 3) & 7;
  const int tid = TID, wave = __builtin_amdgcn_readfirstlane(tid >> 6), lane = tid & 63, l16 = lane * 16;
  STAMPA(1, 0);
  const __amdgpu_buffer_rsrc_t rs = mkrsrc(ws + WS_B, 9 * HOPB), rh = mkrsrc(ws + WS_LIST, HANDB);
  const int c0 = part * 25 + wave, c1 = c0 + 16;
  const bool has1 = wave < 9;
  if (b < NCH) build_lists(ws, b, slice, tid, lane, wave);
  else {
#pragma unroll
    for (int i = 0; i < 7; ++i)
      if (i < 6 || wave < 4) dma1k(ws + WS_B + (size_t)8 * HOPB + slice * SLB + (wave + 16 * i) * 1024 + l16, (wave + 16 * i) * 1024);
    if (tid == 0) *(LAS v4u*)H_SYNC = (v4u){0u, 0u, 0u, 0u};
    WAITVM0();
    LBAR();
    __builtin_amdgcn_s_sleep(48);
  }
  STAMPA(1, 6);
  int n0, n1;
  unsigned tl0;
#pragma unroll 1
  for (unsigned sp = 0;; ++sp) {
    CFENCE();
    const unsigned v = __builtin_amdgcn_raw_buffer_load_b32(rh, lane == 1 && has1 ? 16 * 128 : lane == 2 ? 4 : 0, H_META + c0 * 128, AUX_SC1);
    n0 = __builtin_amdgcn_readlane(v, 0); n1 = __builtin_amdgcn_readlane(v, 1); tl0 = __builtin_amdgcn_readlane(v, 2);
    if (!anyl(v == ~0u) || sp >= SPINCAP) break;
    __builtin_amdgcn_s_sleep(8);
  }
  float d0 = ((const float*)(ws + WS_PDIS))[c0 * 64 + lane];
  float d1 = ((const float*)(ws + WS_PDIS))[(has1 ? c1 : c0) * 64 + (wave == 8 ? lane & 31 : lane)];
  v8bf sel;
  { const int g = lane >> 4, row = lane & 15;
#pragma unroll
    for (int j = 0; j < 8; ++j) sel[j] = (__bf16)(((row >> 2) == 2 * (g & 1) + (j >> 2) && (j & 1) == (row & 1)) ? 1.0f : 0.0f); }
  const int sidx = (lane & 2) ? 0xEEEE : 0x4444;
  wait_until(tl0 + TCL);
  v4u L0[8];
  v2u ov0;
  issue_lists(L0, ov0, rh, c0, n0, l16);
  check_lists(L0, ov0, ws, c0, n0, l16);
  const int o0a = __builtin_amdgcn_readfirstlane(ov0[0]), o0b = __builtin_amdgcn_readfirstlane(ov0[1]);
  d0 *= d0; d1 *= d1;
  STAMPA(1, 7);
  const int tp = b & 7, t0 = (b >> 3) * 782 / 32, nt = ((b >> 3) + 1) * 782 / 32 - t0, pos0 = tp * 1600 + 2 * t0;
  unsigned tspin = 0, tend7;
  v8bf mreg[RF];
  auto hops = [&](auto role) __attribute__((always_inline)) NOPK {
    constexpr bool HAS1 = decltype(role)::v;
    int yo[4];
    v4u L1[8];
    int o1a = 0, o1b = 0;
    if constexpr (HAS1) {
#pragma unroll
      for (int t = 0; t < 4; ++t) {
        const int xk = (t & 1) * 576 + tid, per = t < 2 ? 128 : 144, pj = xk / per;
        yo[t] = xk < 7 * per ? (((part + 1 + pj) & 7) * 25 + (t < 2 ? 16 : 20)) * 512 + (xk - pj * per) * 16 : OOB;
      }
    } else {
      yo[0] = ((part + wave - 8) & 7) * 25 * 512 + l16;
#pragma unroll
      for (int t = 0; t < 2; ++t) {
        const int xk = t * 448 + tid - 576, s = xk / 25;
        yo[1 + t] = xk < 800 ? s * SLB + pos0 * 8 + (xk - s * 25) * 16 : OOB;
      }
    }
    v2u ov1;
    if constexpr (HAS1) issue_lists(L1, ov1, rh, c1, n1, l16);
    STAMPC(1);
    v4u X[8], TK[2];
    v2u mx = {0u, 0u};
    unsigned wv = 0;
#pragma unroll 1
    for (int k = 1;; ++k) {
      if (HAS1 && tid == 0) __builtin_amdgcn_raw_buffer_store_b32(now(), rh, H_FLAG + (((k - 1) * 8 + part) * 32 + slice) * 128, 0, AUX_SC1);
      const int hb = k * HOPB + slice * SLB;
      if (k < 7 && (lane < 32 || HAS1)) store16(((v4u){~0u, ~0u, ~0u, ~0u}), rs, l16 + (lane < 32 ? 0 : 16 * 512 - 512), hb + HOPB + c0 * 512, AUX_SC1);
      v2u r0, r1;
      if constexpr (!HAS1) {
        const int tb = k >= 3 ? (k - 3) * HOPB : OOB;
        TK[0] = load16(rs, yo[1], tb, AUX_SC1);
        TK[1] = load16(rs, yo[2], tb, AUX_SC1);
        if (k == 2 || (k == 3 && wave < 9 + RF - 7)) dma1k(ws + WS_WF + ((k - 2) * 7 + wave - 9) * 1024 + l16, T_PARK + ((k - 2) * 7 + wave - 9) * 1024);
      }
      __builtin_amdgcn_s_setprio(2);
      {
        v4f a = gather(L0, n0, sel, sidx);
        if (o0a | o0b) a = slow_edges(a, ws, c0, o0a, o0b, lane);
        WAITVM0();
        r0 = publish(a, d0, rs, hb + c0 * 512, lane);
      }
      STAMPC(8 * k - 6);
      if constexpr (HAS1) {
        if (k == 1) {
          check_lists(L1, ov1, ws, c1, n1, l16);
          o1a = __builtin_amdgcn_readfirstlane(ov1[0]); o1b = __builtin_amdgcn_readfirstlane(ov1[1]);
        }
        __builtin_amdgcn_s_setprio(0);
        v4f a = gather(L1, n1, sel, sidx);
        __builtin_amdgcn_s_setprio(2);
        if (o1a | o1b) a = slow_edges(a, ws, c1, o1a, o1b, lane);
        r1 = publish(a, d1, rs, hb + c1 * 512, lane);
        if (lane == 0) (void)__hip_atomic_fetch_max((LAS unsigned*)(H_SYNC + 4), now(), __ATOMIC_RELAXED, __HIP_MEMORY_SCOPE_WORKGROUP);
        STAMPC(8 * k - 5);
        if (k == 7) break;
      } else {
        if (k == 7) break;
        if (k >= 3) {
          *(LAS v4u*)(T_TOK + (k + 1) * T_HS + (tid - 576) * 16) = TK[0];
          if (yo[2] != OOB) *(LAS v4u*)(T_TOK + (k + 1) * T_HS + (tid - 576 + 448) * 16) = TK[1];
        }
        if (wave == 9) {
          const unsigned f = wait_flags(rh, lane < 32 ? (((k - 1) * 8 + (lane & 7)) * 32 + slice) * 128 : k >= 3 ? (((k - 2) * 8 + tp) * 32 + (lane & 31)) * 128 : OOB);
          const int lag = wave_max(tdiff(f, __builtin_amdgcn_readlane(f, part)));
          if (lane == 0) *(LAS int*)H_SYNC = lag;
        } else if (k == 1 && wave >= 14) {
          const int q = tid - 896;
          v4f m = {0.f, 0.f, 0.f, 0.f};
          if (q < 96) {
#pragma unroll
            for (int w = 0; w < 8; ++w) m += *(const v4f*)(ws + WS_RS + (((q >> 5) * 8 + w) * 128 + (q & 31) * 4) * 4);
          }
          mx = (v2u){pkbf(m[0], m[1]), pkbf(m[2], m[3])};
          wv = ((const unsigned*)wr)[lane];
        } else __builtin_amdgcn_s_sleep(XSLEEP);
        CFENCE();
#pragma unroll
        for (int i = 0; i < 4; ++i) X[i] = load16(rs, yo[0] + i * 1024, k > 1 ? hb : OOB, AUX_SC1);
        STAMPC(8 * k - 5);
      }
      LBAR();
      STAMPC(8 * k - 4);
      const v2u sy = *(LAS v2u*)H_SYNC;
      const unsigned tend = __builtin_amdgcn_readfirstlane(sy[0] + sy[1]);
      constexpr int NP = HAS1 ? 4 : 8;
      unsigned bad = 0;
      *(LAS v2u*)(c0 * 512 + lane * 8) = r0;
      if constexpr (HAS1) {
        *(LAS v2u*)(c1 * 512 + lane * 8) = r1;
        wait_until(tend + TC1);
        X[0] = load16(rs, yo[0], hb, AUX_SC1);
        X[1] = load16(rs, yo[1], hb, AUX_SC1);
        wait_until(tend + TC2);
        X[2] = load16(rs, yo[2], hb, AUX_SC1);
        X[3] = load16(rs, yo[3], hb, AUX_SC1);
        STAMPC(8 * k - 1);
#pragma unroll
        for (int t = 0; t < 4; ++t) {
          if (max4(X[t]) == ~0u) bad |= 1u << t;
          if (!(t & 1) || yo[t] != OOB) *(LAS v4u*)(yo[t]) = X[t];
          CFENCE();
        }
      } else {
        wait_until(tend - TC0);
        if (k == 1) {
#pragma unroll
          for (int i = 0; i < 4; ++i) X[i] = load16(rs, yo[0] + i * 1024, hb, AUX_SC1);
        }
#pragma unroll
        for (int i = 4; i < 8; ++i) X[i] = load16(rs, yo[0] + i * 1024, hb, AUX_SC1);
        CFENCE();
#pragma unroll
        for (int i = 0; i < 8; ++i) {
          if (max4(X[i]) == ~0u) bad |= 1u << i;
          *(LAS v4u*)(yo[0] + i * 1024) = X[i];
          CFENCE();
        }
        STAMPC(8 * k - 1);
      }
      unsigned spins = 0;
#pragma unroll 1
      while (anyl(bad != 0)) {
        CFENCE();
#pragma unroll
        for (int i = 0; i < NP; ++i) X[i] = load16(rs, (bad >> i) & 1 ? (HAS1 ? yo[i & 3] : yo[0] + i * 1024) : OOB, hb, AUX_SC1);
        ++spins;
#pragma unroll
        for (int i = 0; i < NP; ++i)
          if (((bad >> i) & 1) && (max4(X[i]) != ~0u || spins >= SPINCAP)) { *(LAS v4u*)(HAS1 ? yo[i & 3] : yo[0] + i * 1024) = X[i]; bad ^= 1u << i; }
        if (anyl(bad != 0)) __builtin_amdgcn_s_sleep(4);
      }
      STAMPC(8 * k - 3);
      STAMPS3(STAMPV(4, 8 * k, spins));
      tspin += spins;
      WAITVM0();
      LBAR();
      STAMPC(8 * k - 2);
    }
    LAS char* const pkb = ldsbase(T_PARK + l16);
#pragma unroll
    for (int f = 0; f < RF; ++f) mreg[f] = *(LAS v8bf*)(pkb + f * 1024);
    if constexpr (HAS1) {
      STAMPA(3, 3);
      LBAR();
      { const v2u sy = *(LAS v2u*)H_SYNC; tend7 = __builtin_amdgcn_readfirstlane(sy[0] + sy[1]); }
      STAMPA(3, 5);
#pragma unroll
      for (int i = 0; i < 7; ++i)
        if (wave + 9 * i < 64 - RF) dma1k(ws + WS_WF + (RF + wave + 9 * i) * 1024 + l16, T_W + (wave + 9 * i) * 1024);
    } else {
      const unsigned f = wait_flags(rh, lane < 32 ? ((6 * 8 + tp) * 32 + lane) * 128 : ((6 * 8 + part) * 32 + slice) * 128);
      const int lag = wave_max(tdiff(f, __builtin_amdgcn_readlane(f, 32)));
      if (lane == 0) *(LAS int*)H_SYNC = lag;
#pragma unroll
      for (int i = 0; i < 4; ++i) X[i] = load16(rs, yo[1 + (i & 1)], (5 + (i >> 1)) * HOPB, AUX_SC1);
      STAMPA(3, 4);
      LBAR();
      { const v2u sy = *(LAS v2u*)H_SYNC; tend7 = __builtin_amdgcn_readfirstlane(sy[0] + sy[1]); }
#pragma unroll
      for (int i = 0; i < 6; ++i)
        if (!(i & 1) || yo[2] != OOB) *(LAS v4u*)(T_TOK + (i >> 1) * T_HS + (tid - 576 + (i & 1) * 448) * 16) = i < 2 ? TK[i] : X[i - 2];
      if (wave >= 14) {
        const int q = tid - 896;
        if (q < 96) *(LAS v2u*)(T_X + ((q >> 5) ? (q >> 5) + 2 : 1) * 256 + (q & 31) * 8) = mx;
        *(LAS unsigned*)(T_X + (wave == 14 ? 0 : 512) + lane * 4) = wave == 14 ? 0x3F803F80u : wv;
        float sw = bflo(wv) + bfhi(wv);
        sw += dppf<DPP_X1>(sw); sw += dppf<DPP_X2>(sw); sw += dppf<DPP_HM>(sw); sw += dppf<DPP_RM>(sw);
        sw = add32(add16(sw));
        if (tid == 1023) *(LAS float*)(T_X + 1280) = sw * (1.f / 128.f);
      }
    }
  };
  if (has1) hops(IC<1>{}); else hops(IC<0>{});
  STAMPA(3, 0);
  STAMPV(3, 58, tspin);
  const bool work = 2 * wave < nt;
  const int nmask = 2 * wave + 1 < nt ? 3 : 1;
  const int p = pos0 + (work ? 4 * wave : 0);
  const float ca = ((const float*)(ws + WS_PCI))[p + ((lane >> 3) & nmask)];
  const unsigned ndv = ((const unsigned*)(ws + WS_INV + p * 2))[lane & 1];
  const int s7 = tid / 25, o7 = tid < 800 ? s7 * SLB + pos0 * 8 + (tid - s7 * 25) * 16 : OOB;
  wait_until(tend7 + TC2);
  STAMPA(3, 6);
  {
    v4u y = load16(rs, o7, 7 * HOPB, AUX_SC1);
#pragma unroll 1
    for (unsigned sp = 0; sp < SPINCAP; ++sp) {
      const bool bd = max4(y) == ~0u;
      if (!anyl(bd)) break;
      __builtin_amdgcn_s_sleep(4);
      CFENCE();
      if (bd) y = load16(rs, o7, 7 * HOPB, AUX_SC1);
    }
    STAMPA(3, 7);
    if (tid < 800) *(LAS v4u*)(T_TOK + 3 * T_HS + tid * 16) = y;
  }
  WAITVM0();
  LBAR();
  STAMPA(3, 1); STAMPV(3, 60, __builtin_amdgcn_s_memtime());
  if (work) tf_batch(wave, nmask, mreg, (lane & 7) ? ca : 1.f, __builtin_amdgcn_readlane(ndv, 0), nmask == 3 ? __builtin_amdgcn_readlane(ndv, 1) : ~0u, ws, out, lane);
  STAMPA(3, 2); STAMPV(3, 61, __builtin_amdgcn_s_memtime());
}

extern "C" void kernel_launch(void* const* d_in, const int* in_sizes, int n_in, void* d_out, int out_size, void* d_ws, size_t ws_size, hipStream_t stream) {
  (void)in_sizes; (void)n_in; (void)out_size; (void)ws_size;
  char* ws = (char*)d_ws;
  (void)hipFuncSetAttribute((const void*)k_prep, hipFuncAttributeMaxDynamicSharedMemorySize, K1_LDS);
  (void)hipFuncSetAttribute((const void*)k_main, hipFuncAttributeMaxDynamicSharedMemorySize, KM_LDS);
  k_prep<<<G1 + 8, 1024, K1_LDS, stream>>>((const u16*)d_in[0], (const int*)d_in[1], (const int*)d_in[2], (const u16*)d_in[3],
                                           (const u16*)d_in[4], (const u16*)d_in[5], (const u16*)d_in[6], (const u16*)d_in[7], (const u16*)d_in[8], ws);
  k_main<<<256, 1024, KM_LDS, stream>>>(ws, (const u16*)d_in[8], (u16*)d_out);
}
```

```cpp
#include <hip/hip_runtime.h>
#include <stdint.h>

typedef unsigned short u16;
typedef unsigned char u8;
typedef unsigned long long u64;
typedef __bf16 v8bf __attribute__((ext_vector_type(8)));
typedef __bf16 v4bf __attribute__((ext_vector_type(4)));
typedef __bf16 v2bf __attribute__((ext_vector_type(2)));
typedef float v4f __attribute__((ext_vector_type(4)));
typedef float v2f __attribute__((ext_vector_type(2)));
typedef float v16f __attribute__((ext_vector_type(16)));
typedef unsigned v4u __attribute__((ext_vector_type(4)));
typedef unsigned v2u __attribute__((ext_vector_type(2)));
typedef int v4i __attribute__((ext_vector_type(4)));
typedef short v4s __attribute__((ext_vector_type(4)));
typedef u16 v2h __attribute__((ext_vector_type(2)));
#define LAS __attribute__((address_space(3)))
#define GAS __attribute__((address_space(1)))
#if defined(__HIP_DEVICE_COMPILE__)
#define NOPK __attribute__((target("no-packed-fp32-ops")))
#else
#define NOPK
#endif
#define DEVI __device__ __forceinline__ NOPK
#define RARE(x) __builtin_expect(!!(x), 0)
#define TID ((int)__builtin_amdgcn_workitem_id_x())
#define BID ((int)__builtin_amdgcn_workgroup_id_x())
DEVI float u2f(unsigned u) { return __builtin_bit_cast(float, u); }
DEVI unsigned f2u(float f) { return __builtin_bit_cast(unsigned, f); }
DEVI unsigned umin(unsigned a, unsigned b) { return a < b ? a : b; }
DEVI unsigned umax(unsigned a, unsigned b) { return a > b ? a : b; }
DEVI void wgsync() { __builtin_amdgcn_fence(__ATOMIC_RELEASE, "workgroup"); __builtin_amdgcn_s_barrier(); __builtin_amdgcn_fence(__ATOMIC_ACQUIRE, "workgroup"); }

constexpr int NN = 12500, NE = 400000;
constexpr int NCH = 200;
constexpr int NPOS = NCH * 64;
constexpr int SLB = NPOS * 8;
constexpr int HOPB = 32 * SLB;
constexpr int G1 = 224, CAP = 32, EPW = 893, NODEWG = 196;
constexpr int SEGB = G1 * CAP * 4;
constexpr size_t WS_B = 0;
constexpr size_t WS_SEG = WS_B + (size_t)8 * HOPB;
constexpr size_t WS_LIST = WS_SEG + (size_t)NCH * SEGB;
constexpr size_t WS_META = WS_LIST + (size_t)NCH * 8192;
constexpr int H_META = NCH * 8192, H_FLAG = H_META + NCH * 128;
constexpr int HANDB = H_FLAG + 7 * 256 * 128;
constexpr size_t WS_WF = WS_LIST + HANDB;
constexpr size_t WS_RS = WS_WF + 65536;
constexpr size_t WS_INV = WS_RS + 12288;
constexpr size_t WS_PDIS = WS_INV + 25600;
constexpr size_t WS_PCI = WS_PDIS + 51200;
constexpr size_t WS_PSH = WS_PCI + 51200;
constexpr size_t WS_O1C = WS_PSH + 51200;
constexpr size_t WS_O1 = WS_O1C + 1024;
constexpr size_t WS_O2 = WS_O1 + (size_t)G1 * 2048 * 4;
constexpr size_t WS_DBG = WS_O2 + (size_t)NCH * SEGB;

#define SPINCAP (1u << 22)
#define STAMPV(kern, i, v) do {} while (0)
#define STAMPA(kern, i) do {} while (0)
#define STAMPG(kern, i) do {} while (0)
#define STAMPC(i) do {} while (0)
#define STAMPS3(x)

DEVI LAS char* ldsbase(int a) { asm volatile("" : "+v"(a)); return (LAS char*)a; }
DEVI float bflo(unsigned u) { return u2f(u << 16); }
DEVI float bfhi(unsigned u) { return u2f(u & 0xffff0000u); }
DEVI unsigned pkbf(float a, float b) { v2f v = {a, b}; return __builtin_bit_cast(unsigned, __builtin_convertvector(v, v2bf)); }
DEVI __amdgpu_buffer_rsrc_t mkrsrc(const void* p, unsigned bytes) { return __builtin_amdgcn_make_buffer_rsrc((void*)p, 0, bytes, 0x00020000); }
#define store16(v, rs, voff, soff, aux) __builtin_amdgcn_raw_buffer_store_b128(v, rs, (voff) + (soff), 0, aux)
template <int C> DEVI unsigned dppu(unsigned x) { return (unsigned)__builtin_amdgcn_update_dpp(0, (int)x, C, 0xF, 0xF, true); }
template <int C> DEVI float dppf(float x) { return u2f(dppu<C>(f2u(x))); }
constexpr int DPP_X1 = 0xB1, DPP_X2 = 0x4E, DPP_HM = 0x141, DPP_RM = 0x140;
DEVI float add16(float x) { v2u r = __builtin_amdgcn_permlane16_swap(f2u(x), f2u(x), false, false); return u2f(r[0]) + u2f(r[1]); }
DEVI float add32(float x) { v2u r = __builtin_amdgcn_permlane32_swap(f2u(x), f2u(x), false, false); return u2f(r[0]) + u2f(r[1]); }
DEVI unsigned or16(unsigned x) { v2u r = __builtin_amdgcn_permlane16_swap(x, x, false, false); return r[0] | r[1]; }

DEVI bool anyl(bool c) { return __builtin_amdgcn_ballot_w64(c) != 0; }
constexpr unsigned LGRAN = 4;
template <int Q> DEVI void touch_code(long long pc, int tid, int bytes) {
  const __amdgpu_buffer_rsrc_t rc = mkrsrc((const void*)(pc & ~255ll), bytes);
  v4u v[Q];
#pragma unroll
  for (int i = 0; i < Q; ++i) v[i] = __builtin_amdgcn_raw_buffer_load_b128(rc, tid * 16, i * 16384, 0);
#pragma unroll
  for (int i = 0; i < Q; ++i) asm volatile("" :: "v"(v[i]));
}
template <int I> struct IC { static constexpr int v = I; };
DEVI unsigned key2(unsigned d, unsigned idx2) {
  v2h v = __builtin_bit_cast(v2h, d);
  v2h t = __builtin_elementwise_sub_sat((v2h){0x3F80, 0x3F80}, v);
  t = __builtin_elementwise_min(t, (v2h){4095, 4095});
  return (__builtin_bit_cast(unsigned, t) << 3) | idx2;
}
DEVI v4u keys8(v4u d) { return (v4u){key2(d[0], 0x00010000u), key2(d[1], 0x00030002u), key2(d[2], 0x00050004u), key2(d[3], 0x00070006u)}; }
DEVI unsigned rank8(v4u k, unsigned own) {
  const v2h o = __builtin_bit_cast(v2h, own | (own << 16));
  unsigned t[4];
#pragma unroll
  for (int i = 0; i < 4; ++i) { const unsigned ki = k[i]; const v2h df = __builtin_bit_cast(v2h, ki) - o; t[i] = __builtin_bit_cast(unsigned, df); }
  const unsigned a = __builtin_amdgcn_perm(t[0], t[1], 0x07050301u) & 0x80808080u;
  const unsigned b = __builtin_amdgcn_perm(t[2], t[3], 0x07050301u) & 0x80808080u;
  return __builtin_popcount(a) + __builtin_popcount(b);
}
DEVI unsigned chunk_of(unsigned w, unsigned r) {
  const u64 t0 = 0x1511090A140C0100ull, t1 = 0x16120D0B06100204ull, t2 = 0x17130E0F07050308ull;
  const unsigned j = w >> 3;
  const u64 t = j == 0 ? t0 : j == 1 ? t1 : t2;
  const unsigned slot = j == 3 ? 24u : (unsigned)(t >> (8 * r)) & 31u;
  return ((w + r) & 7) * 25 + slot;
}

#define B1AUX 16
#define K1AUX 0
constexpr int LDS_NONE = 0x7FFF0;
constexpr int OOB = 0x40000000;
constexpr int AUX_SC1 = 16;
#define WAITVM0() __builtin_amdgcn_s_waitcnt(0x0F70)
#define WAITLG0() __builtin_amdgcn_s_waitcnt(0xC07F)
#define CFENCE() asm volatile("" ::: "memory")
#define LBAR() do { CFENCE(); WAITLG0(); __builtin_amdgcn_s_barrier(); CFENCE(); } while (0)
#define load16(rs, voff, soff, aux) __builtin_amdgcn_raw_buffer_load_b128(rs, voff, soff, aux)
constexpr int L1_LUT = 0, L1_CNT = 512, L1_ROW = L1_CNT + 1024, L1_D = L1_ROW + 64, L1_KEY = 2560, L1_SEG = L1_KEY + 25088, L1_X = L1_SEG + NCH * CAP * 4, K1_LDS = L1_X + 64 * 272;
#define LDS_ADD(off, v) __hip_atomic_fetch_add((LAS unsigned*)(off), v, __ATOMIC_RELAXED, __HIP_MEMORY_SCOPE_WORKGROUP)
#define LDS_OR(off, v) (void)__hip_atomic_fetch_or((LAS unsigned*)(off), v, __ATOMIC_RELAXED, __HIP_MEMORY_SCOPE_WORKGROUP)
__global__ void __launch_bounds__(1024, 4) k_prep(const u16* __restrict__ x, const int* __restrict__ src, const int* __restrict__ dst, const u16* __restrict__ dis,
                                                  const u16* __restrict__ wq, const u16* __restrict__ wk, const u16* __restrict__ wv, const u16* __restrict__ wo, const u16* __restrict__ wr, char* __restrict__ ws, const u16* __restrict__ out) {
  extern __shared__ __attribute__((aligned(16))) char lds[];
  const int tid = TID, b = BID;
  const int swave_ = __builtin_amdgcn_readfirstlane(tid >> 6); (void)swave_;
  STAMPA(0, 0);
  if (__builtin_expect(b < G1, 1)) {
    const __amdgpu_buffer_rsrc_t rdis = mkrsrc(dis, NN * 2), rx = mkrsrc(x, NN * 256), rb = mkrsrc(ws + WS_B, 2 * HOPB), rsg = mkrsrc(ws + WS_SEG, NCH * SEGB);
    const int bp = (b & ~15) | ((b & 7) << 1) | ((b >> 3) & 1);
    const int n0 = bp * 64, t16 = tid * 16, wave = __builtin_amdgcn_readfirstlane(tid >> 6);
    const bool act = tid < EPW && b * EPW + tid < NE / 2;
    const v4u kd0 = __builtin_amdgcn_raw_buffer_load_b128(rdis, t16, 0, 0), kd1 = __builtin_amdgcn_raw_buffer_load_b128(rdis, t16, 16384, 0);
    const v4u xv = __builtin_amdgcn_raw_buffer_load_b128(rx, t16, n0 * 256, 0);
    const v2u d2 = __builtin_bit_cast(v2u, __builtin_amdgcn_raw_buffer_load_b64(mkrsrc(dst, NE * 4), tid * 8, b * EPW * 8, 0));
    const v2u s2 = __builtin_bit_cast(v2u, __builtin_amdgcn_raw_buffer_load_b64(wave < 14 ? mkrsrc(src, NE * 4) : rdis, wave < 14 ? tid * 8 : (tid & 124), wave < 14 ? b * EPW * 8 : n0 * 2, 0));
    const unsigned dn = s2[0];
    {
      const v4u s = {~0u, ~0u, ~0u, ~0u};
      *(LAS v4u*)(L1_SEG + t16) = s;
      if (tid < NCH * CAP / 4 - 1024) *(LAS v4u*)(L1_SEG + 16384 + t16) = s;
    }
    if (tid < 256) { *(LAS unsigned*)(L1_CNT + tid * 4) = 0; *(LAS u16*)(L1_LUT + tid * 2) = (u16)(chunk_of(tid >> 3, tid & 7) * 64); }
    *(LAS v4u*)(L1_KEY + t16) = keys8(kd0);
    if (tid < 1568 - 1024) *(LAS v4u*)(L1_KEY + 16384 + t16) = keys8(tid < 512 ? kd1 : (v4u){0u, 0u, 0u, 0u});
    *(LAS v4u*)(L1_X + (tid >> 4) * 272 + (tid & 15) * 16) = xv;
    LBAR();
    STAMPG(0, 1);
    if (wave >= 14 && bp < NODEWG) {
      __builtin_amdgcn_s_setprio(2);
      const int i = (tid - 896) >> 1, n = n0 + i;
      float s = 0.f, q = 0.f;
#pragma unroll
      for (int j = 0; j < 8; ++j) {
        const v4u w = *(LAS v4u*)(L1_X + i * 272 + (tid & 1) * 128 + j * 16);
#pragma unroll
        for (int k = 0; k < 4; ++k) {
          const unsigned u = w[k];
          const v2bf pr = __builtin_bit_cast(v2bf, u);
          s = __builtin_amdgcn_fdot2_f32_bf16(pr, (v2bf){(__bf16)1.0f, (__bf16)1.0f}, s, false);
          q = __builtin_amdgcn_fdot2_f32_bf16(pr, pr, q, false);
        }
      }
      s += dppf<DPP_X1>(s); q += dppf<DPP_X1>(q);
      const unsigned r = rank8(*(LAS v4u*)(L1_KEY + (n >> 3) * 16), *(LAS u16*)(L1_KEY + n * 2));
      const unsigned pos = chunk_of(n0 >> 9, r) * 64 + ((n >> 3) & 63);
      const float dv = (i & 1) ? bfhi(dn) : bflo(dn);
      *(LAS u8*)(L1_ROW + r * 8 + (i >> 3)) = (u8)i;
      *(LAS v2f*)(L1_D + (r * 8 + (i >> 3)) * 8) = (v2f){dv, s * dv * (-15.f / 2048.f)};
      if (tid & 1) {
        ((u16*)(ws + WS_INV))[pos] = n < NN ? (u16)n : (u16)0xFFFF;
        ((float*)(ws + WS_PDIS))[pos] = dv;
      } else {
        ((float*)(ws + WS_PCI))[pos] = dv == 0.f ? 1.f : 1.f / dv;
        ((float*)(ws + WS_PSH))[pos] = s * s > 25.6f * q ? s * (1.f / 128.f) : 0.f;
      }
    }
    if (act) {
      const unsigned nd[4] = {d2[0], d2[1], s2[0], s2[1]};
      v4u kg[4];
      unsigned ko[4], ps[4], sl[2];
#pragma unroll
      for (int i = 0; i < 4; ++i) { kg[i] = *(LAS v4u*)(L1_KEY + (nd[i] >> 3) * 16); ko[i] = *(LAS u16*)(L1_KEY + nd[i] * 2); }
      __builtin_amdgcn_sched_barrier(0);
#pragma unroll
      for (int i = 0; i < 4; ++i) ps[i] = *(LAS u16*)(L1_LUT + ((nd[i] >> 9) * 8 + rank8(kg[i], ko[i])) * 2);
      __builtin_amdgcn_sched_barrier(0);
#pragma unroll
      for (int i = 0; i < 2; ++i) sl[i] = LDS_ADD(L1_CNT + (ps[i] >> 4), 1u);
      unsigned en[2];
#pragma unroll
      for (int i = 0; i < 2; ++i) {
        const unsigned x3 = nd[i] ^ nd[2 + i];
        ps[2 + i] |= (nd[2 + i] >> 3) & 63;
        en[i] = ((x3 & 0xF0) << 19) | ((nd[i] & 0x1F8) << 14) | ((x3 & 8) << 13) | ps[2 + i];
      }
#pragma unroll
      for (int i = 0; i < 2; ++i) *(LAS unsigned*)(sl[i] < CAP ? L1_SEG + ps[i] * (CAP / 16) + sl[i] * 4 : LDS_NONE) = en[i];
      if (RARE(umax(sl[0], sl[1]) >= CAP)) {
#pragma unroll
        for (int i = 0; i < 2; ++i)
          if (sl[i] >= CAP) ((unsigned*)(ws + WS_O1))[b * 2048 + LDS_ADD(L1_CNT + 1020, 1u)] = ((ps[i] | ((nd[i] >> 3) & 63)) << 14) | ps[2 + i];
      }
    }
    LBAR();
    STAMPG(0, 2);
    {
      const int c = tid >> 2, so = c * SEGB + (tid & 3) * 16 + bp * 64, lo = L1_SEG + c * 128 + (tid & 3) * 16;
      if (tid < NCH * 4) {
        store16(*(LAS v4u*)lo, rsg, so, 0, 0);
        if (*(LAS unsigned*)(L1_CNT + c * 4) >= 16) store16(*(LAS v4u*)(lo + 64), rsg, so, SEGB / 2, 0);
      }
    }
    if (tid == 0) ((unsigned*)(ws + WS_O1C))[b] = *(LAS unsigned*)(L1_CNT + 1020);
    if (bp < NODEWG) {
      const int u = tid & 3, s = (tid >> 2) & 31, r = wave >> 1;
      const unsigned rr = *(LAS u16*)(L1_ROW + r * 8 + 2 * u);
      const v4f dd = *(LAS v4f*)(L1_D + r * 64 + u * 16);
      const v2u a = *(LAS v2u*)(L1_X + (rr & 0xff) * 272 + s * 8), c = *(LAS v2u*)(L1_X + (rr >> 8) * 272 + s * 8);
      const unsigned off = s * SLB + u * 16 + (chunk_of(n0 >> 9, r) * 64 + ((n0 >> 3) & 63)) * 8;
#define SC_(v, d, m) pkbf(__builtin_fmaf(bflo(v), d, m), __builtin_fmaf(bfhi(v), d, m))
      store16(((v4u){SC_(a[0], dd[0], dd[1]), SC_(a[1], dd[0], dd[1]), SC_(c[0], dd[2], dd[3]), SC_(c[1], dd[2], dd[3])}), rb, off, 0, K1AUX);
    }
    STAMPA(0, 4);
  } else if (b >= G1 + 8) {
    const __amdgpu_buffer_rsrc_t rb = mkrsrc(ws + WS_B + HOPB, HOPB), rh = mkrsrc(ws + WS_LIST + H_META, HANDB - H_META);
    const int o = (b - G1 - 8) * 16384 + tid * 16;
    const unsigned touch = __builtin_amdgcn_raw_buffer_load_b32(__builtin_amdgcn_readfirstlane(tid) < 960 ? mkrsrc(ws, WS_O1) : mkrsrc(out, NN * 256), (tid < 960 ? tid : tid - 960) * 65536, 0, 0);
    store16(((v4u){~0u, ~0u, ~0u, ~0u}), rh, o, 0, B1AUX);
#pragma unroll 1
    for (int i = o; i < HOPB; i += 24 * 16384) store16(((v4u){~0u, ~0u, ~0u, ~0u}), rb, i, 0, B1AUX);
    asm volatile("" :: "v"(touch));
  } else {
    const int v = b - G1, which = __builtin_amdgcn_readfirstlane(tid >> 9), rt = (tid >> 6) & 7, lane = tid & 63, g = lane >> 4, c = lane & 15;
    const int e = 16 * v + (which == 0 ? (c & 3) | ((c & 4) << 1) | ((c & 8) >> 1) : c);
    const u16* wa = which == 0 ? wq : wv;
    v8bf af[4], bf[4];
#pragma unroll
    for (int ks = 0; ks < 4; ++ks) af[ks] = *(const v8bf*)(wa + (16 * rt + c) * 128 + 32 * ks + 8 * g);
    const float we = bflo(wr[e]);
    {
      const int t = tid & 255;
      if ((tid & 511) < 256) {
        if (which == 0) *(LAS v4u*)(2048 + (t >> 4) * 272 + (t & 15) * 16) = *(const v4u*)(wk + (16 * v + (t >> 4)) * 128 + (t & 15) * 8);
        else *(LAS v4u*)(8192 + (t >> 1) * 48 + (t & 1) * 16) = *(const v4u*)(wo + (t >> 1) * 128 + 16 * v + (t & 1) * 8);
      }
      LBAR();
#pragma unroll
      for (int ks = 0; ks < 4; ++ks) {
        if (which == 0) bf[ks] = *(LAS v8bf*)(2048 + (e & 15) * 272 + 64 * ks + 16 * g);
        else {
          LAS char* q = (LAS char*)(8192 + (32 * ks + 8 * g + (c >> 2)) * 48 + (c & 3) * 8);
          bf[ks] = __builtin_shufflevector(__builtin_amdgcn_ds_read_tr16_b64_v4bf16((LAS v4bf*)q), __builtin_amdgcn_ds_read_tr16_b64_v4bf16((LAS v4bf*)(q + 4 * 48)), 0, 1, 2, 3, 4, 5, 6, 7);
        }
      }
    }
    v4f acc = {0, 0, 0, 0};
#pragma unroll
    for (int ks = 0; ks < 4; ++ks) acc = __builtin_amdgcn_mfma_f32_16x16x32_bf16(af[ks], bf[ks], acc, 0, 0, 0);
    const float cs = add32(add16((acc[0] + acc[1]) + (acc[2] + acc[3])));
    float* csL = (float*)lds;
    if (lane < 16) csL[(which * 16 + c) * 8 + rt] = cs;
    __syncthreads();
    const v4f p0 = *(const v4f*)(csL + (which * 16 + c) * 8), p1 = *(const v4f*)(csL + (which * 16 + c) * 8 + 4);
    acc -= (((p0[0] + p0[1]) + (p0[2] + p0[3])) + ((p1[0] + p1[1]) + (p1[2] + p1[3]))) * (1.f / 128.f);
    if (which == 0) acc *= 1.4426950408889634f * 0.08838834764831845f;
    *(v2u*)(ws + WS_WF + which * 32768 + (((v >> 1) * 8 + rt) * 64 + 32 * (g >> 1) + 16 * (v & 1) + c) * 16 + 8 * (g & 1)) = (v2u){pkbf(acc[0], acc[1]), pkbf(acc[2], acc[3])};
    v4f t;
#pragma unroll
    for (int r = 0; r < 4; ++r) {
      const float u = acc[r] * we;
      float x0 = (c & 1) ? u : acc[r];
      x0 += dppf<DPP_X1>((c & 1) ? acc[r] : u); x0 += dppf<DPP_X2>(x0); x0 += dppf<0x124>(x0); x0 += dppf<0x128>(x0);
      t[r] = x0;
    }
    if (c <= which) *(v4f*)(ws + WS_RS + (((which + c) * 8 + v) * 128 + 16 * rt + 4 * g) * 4) = t;
    STAMPA(0, 9);
  }
}

constexpr int KM_LDS = 163840, TB = KM_LDS - SLB;
constexpr int L2_CNT = 0, L2_MSK = 8192, L2_OUT = L2_MSK + 2048 + 64, L2_PICK = L2_OUT + 16384, L2_EXT = L2_PICK + 16384, EXTCAP = 4096;
constexpr int M_E2 = L2_MSK + 512, M_BK = L2_MSK + 768, M_DG = L2_MSK + 1024, M_LO = L2_MSK + 1280, M_FR = L2_MSK + 1536, M_OV = L2_MSK + 2048;
constexpr int RF = 9;
constexpr int T_HS = 12832, T_TOK = 0, T_PARK = 4 * T_HS, T_W = 8 * T_HS, T_SCR = T_W + (64 - RF) * 1024, T_X = T_SCR + 13 * 256, T_HAND = T_X + 1288;
constexpr int H_SYNC = T_PARK + RF * 1024;
static_assert(TB == 61440 && T_HAND + 200 <= KM_LDS && H_SYNC + 16 <= TB && L2_EXT + EXTCAP * 4 <= H_SYNC, "LDS map");
DEVI unsigned max4(v4u v) { return umax(umax(v[0], v[1]), umax(v[2], v[3])); }
#define NAPN 15
DEVI unsigned now() { return (unsigned)__builtin_amdgcn_s_memrealtime() & 0x7FFFFFFFu; }
DEVI int tdiff(unsigned a, unsigned b) { return (int)((a - b) << 1) >> 1; }
DEVI void wait_until(unsigned t) {
#pragma unroll 1
  for (int i = 0; i < 256 && tdiff(now(), t) < 0; ++i) __builtin_amdgcn_s_sleep(1);
  CFENCE();
}
DEVI void nap(int ticks) {
#pragma unroll 1
  for (int i = umin(ticks, 2048) * NAPN; i > 0; i -= 64) __builtin_amdgcn_s_sleep(1);
  CFENCE();
}
DEVI int imax(int a, int b) { return a > b ? a : b; }
DEVI int wave_max(int v) {
  v = imax(v, (int)dppu<DPP_X1>(v)); v = imax(v, (int)dppu<DPP_X2>(v)); v = imax(v, (int)dppu<DPP_HM>(v)); v = imax(v, (int)dppu<DPP_RM>(v));
  return imax(__builtin_amdgcn_readlane(v, 0), __builtin_amdgcn_readlane(v, 16));
}
DEVI void dma1k(const char* gsrc, int ldsdst) {
  unsigned keep;
  asm volatile("s_mov_b32 %0, m0\n\ts_mov_b32 m0, %2\n\ts_nop 0\n\tglobal_load_lds_dwordx4 %1, off\n\ts_mov_b32 m0, %0" : "=&s"(keep) : "v"(gsrc), "s"(ldsdst) : "memory");
}
DEVI int kth_high(v2u m, unsigned k) {
  const unsigned ch = __builtin_popcount(m[1]);
  const bool hi = k < ch;
  unsigned w = hi ? m[1] : m[0];
  k = hi ? k : k - ch;
  if (k >= (unsigned)__builtin_popcount(w)) return -1;
  int pos = hi ? 32 : 0;
#pragma unroll
  for (int sh = 16; sh >= 1; sh >>= 1) {
    const unsigned up = w >> sh, cu = __builtin_popcount(up);
    const bool t = k < cu;
    w = t ? up : w & ((1u << sh) - 1u);
    k = t ? k : k - cu;
    pos += t ? sh : 0;
  }
  return pos;
}

#define COLOUR_ASM \
  "v_bfe_u32 v11, %2, 0, 1\n" \
  "v_and_b32 v8, %1, %0\n" \
  "v_mov_b32 v9, %1\n" \
  "v_sub_u32 v10, v8, v11\n" \
  "v_bitop3_b32 %1, v9, v8, v10 bitop3:0xb4\n" \
  "v_bitop3_b32 %0, %0, v8, v10 bitop3:0xb4\n" \
  "v_bitop3_b32 v12, v8, v10, v10 bitop3:0x30\n" \
  "v_bfe_u32 v11, %2, 1, 1\n" \
  "v_and_b32_dpp v8, %1, %0 quad_perm:[1,0,3,2] row_mask:0xf bank_mask:0xf bound_ctrl:0\n" \
  "v_mov_b32_dpp v9, %1 quad_perm:[1,0,3,2] row_mask:0xf bank_mask:0xf bound_ctrl:0\n" \
  "v_sub_u32 v10, v8, v11\n" \
  "v_bitop3_b32 %1, v9, v8, v10 bitop3:0xb4\n" \
  "v_bitop3_b32 %0, %0, v8, v10 bitop3:0xb4\n" \
  "v_bitop3_b32 v13, v8, v10, v10 bitop3:0x30\n" \
  "v_bfe_u32 v11, %2, 3, 1\n" \
  "ds_write_b64 %3, v[12:13] offset:0\n" \
  "v_and_b32_dpp v8, %1, %0 quad_perm:[2,3,0,1] row_mask:0xf bank_mask:0xf bound_ctrl:0\n" \
  "v_mov_b32_dpp v9, %1 quad_perm:[2,3,0,1] row_mask:0xf bank_mask:0xf bound_ctrl:0\n" \
  "v_sub_u32 v10, v8, v11\n" \
  "v_bitop3_b32 %1, v9, v8, v10 bitop3:0xb4\n" \
  "v_bitop3_b32 %0, %0, v8, v10 bitop3:0xb4\n" \
  "v_bitop3_b32 v13, v8, v10, v10 bitop3:0x30\n" \
  "v_bfe_u32 v11, %2, 2, 1\n" \
  "v_and_b32_dpp v8, %1, %0 quad_perm:[1,0,3,2] row_mask:0xf bank_mask:0xf bound_ctrl:0\n" \
  "v_mov_b32_dpp v9, %1 quad_perm:[1,0,3,2] row_mask:0xf bank_mask:0xf bound_ctrl:0\n" \
  "v_sub_u32 v10, v8, v11\n" \
  "v_bitop3_b32 %1, v9, v8, v10 bitop3:0xb4\n" \
  "v_bitop3_b32 %0, %0, v8, v10 bitop3:0xb4\n" \
  "v_bitop3_b32 v12, v8, v10, v10 bitop3:0x30\n" \
  "v_bfe_u32 v11, %2, 5, 1\n" \
  "ds_write_b64 %3, v[12:13] offset:512\n" \
  "v_and_b32_dpp v8, %1, %0 row_half_mirror row_mask:0xf bank_mask:0xf bound_ctrl:0\n" \
  "v_mov_b32_dpp v9, %1 row_half_mirror row_mask:0xf bank_mask:0xf bound_ctrl:0\n" \
  "v_sub_u32 v10, v8, v11\n" \
  "v_bitop3_b32 %1, v9, v8, v10 bitop3:0xb4\n" \
  "v_bitop3_b32 %0, %0, v8, v10 bitop3:0xb4\n" \
  "v_bitop3_b32 v13, v8, v10, v10 bitop3:0x30\n" \
  "v_bfe_u32 v11, %2, 4, 1\n" \
  "v_and_b32_dpp v8, %1, %0 quad_perm:[1,0,3,2] row_mask:0xf bank_mask:0xf bound_ctrl:0\n" \
  "v_mov_b32_dpp v9, %1 quad_perm:[1,0,3,2] row_mask:0xf bank_mask:0xf bound_ctrl:0\n" \
  "v_sub_u32 v10, v8, v11\n" \
  "v_bitop3_b32 %1, v9, v8, v10 bitop3:0xb4\n" \
  "v_bitop3_b32 %0, %0, v8, v10 bitop3:0xb4\n" \
  "v_bitop3_b32 v12, v8, v10, v10 bitop3:0x30\n" \
  "v_bfe_u32 v11, %2, 6, 1\n" \
  "ds_write_b64 %3, v[12:13] offset:1024\n" \
  "v_and_b32_dpp v8, %1, %0 quad_perm:[2,3,0,1] row_mask:0xf bank_mask:0xf bound_ctrl:0\n" \
  "v_mov_b32_dpp v9, %1 quad_perm:[2,3,0,1] row_mask:0xf bank_mask:0xf bound_ctrl:0\n" \
  "v_sub_u32 v10, v8, v11\n" \
  "v_bitop3_b32 %1, v9, v8, v10 bitop3:0xb4\n" \
  "v_bitop3_b32 %0, %0, v8, v10 bitop3:0xb4\n" \
  "v_bitop3_b32 v12, v8, v10, v10 bitop3:0x30\n" \
  "v_bfe_u32 v11, %2, 7, 1\n" \
  "v_and_b32_dpp v8, %1, %0 quad_perm:[1,0,3,2] row_mask:0xf bank_mask:0xf bound_ctrl:0\n" \
  "v_mov_b32_dpp v9, %1 quad_perm:[1,0,3,2] row_mask:0xf bank_mask:0xf bound_ctrl:0\n" \
  "v_sub_u32 v10, v8, v11\n" \
  "v_bitop3_b32 %1, v9, v8, v10 bitop3:0xb4\n" \
  "v_bitop3_b32 %0, %0, v8, v10 bitop3:0xb4\n" \
  "v_bitop3_b32 v13, v8, v10, v10 bitop3:0x30\n" \
  "v_bfe_u32 v11, %2, 8, 1\n" \
  "ds_write_b64 %3, v[12:13] offset:1536\n" \
  "v_and_b32_dpp v8, %1, %0 row_mirror row_mask:0xf bank_mask:0xf bound_ctrl:0\n" \
  "v_mov_b32_dpp v9, %1 row_mirror row_mask:0xf bank_mask:0xf bound_ctrl:0\n" \
  "v_sub_u32 v10, v8, v11\n" \
  "v_bitop3_b32 %1, v9, v8, v10 bitop3:0xb4\n" \
  "v_bitop3_b32 %0, %0, v8, v10 bitop3:0xb4\n" \
  "v_bitop3_b32 v12, v8, v10, v10 bitop3:0x30\n" \
  "v_bfe_u32 v11, %2, 9, 1\n" \
  "v_and_b32_dpp v8, %1, %0 quad_perm:[1,0,3,2] row_mask:0xf bank_mask:0xf bound_ctrl:0\n" \
  "v_mov_b32_dpp v9, %1 quad_perm:[1,0,3,2] row_mask:0xf bank_mask:0xf bound_ctrl:0\n" \
  "v_sub_u32 v10, v8, v11\n" \
  "v_bitop3_b32 %1, v9, v8, v10 bitop3:0xb4\n" \
  "v_bitop3_b32 %0, %0, v8, v10 bitop3:0xb4\n" \
  "v_bitop3_b32 v13, v8, v10, v10 bitop3:0x30\n" \
  "v_bfe_u32 v11, %2, 11, 1\n" \
  "ds_write_b64 %3, v[12:13] offset:2048\n" \
  "v_and_b32_dpp v8, %1, %0 quad_perm:[2,3,0,1] row_mask:0xf bank_mask:0xf bound_ctrl:0\n" \
  "v_mov_b32_dpp v9, %1 quad_perm:[2,3,0,1] row_mask:0xf bank_mask:0xf bound_ctrl:0\n" \
  "v_sub_u32 v10, v8, v11\n" \
  "v_bitop3_b32 %1, v9, v8, v10 bitop3:0xb4\n" \
  "v_bitop3_b32 %0, %0, v8, v10 bitop3:0xb4\n" \
  "v_bitop3_b32 v13, v8, v10, v10 bitop3:0x30\n" \
  "v_bfe_u32 v11, %2, 10, 1\n" \
  "v_and_b32_dpp v8, %1, %0 quad_perm:[1,0,3,2] row_mask:0xf bank_mask:0xf bound_ctrl:0\n" \
  "v_mov_b32_dpp v9, %1 quad_perm:[1,0,3,2] row_mask:0xf bank_mask:0xf bound_ctrl:0\n" \
  "v_sub_u32 v10, v8, v11\n" \
  "v_bitop3_b32 %1, v9, v8, v10 bitop3:0xb4\n" \
  "v_bitop3_b32 %0, %0, v8, v10 bitop3:0xb4\n" \
  "v_bitop3_b32 v12, v8, v10, v10 bitop3:0x30\n" \
  "v_bfe_u32 v11, %2, 13, 1\n" \
  "ds_write_b64 %3, v[12:13] offset:2560\n" \
  "v_and_b32_dpp v8, %1, %0 row_half_mirror row_mask:0xf bank_mask:0xf bound_ctrl:0\n" \
  "v_mov_b32_dpp v9, %1 row_half_mirror row_mask:0xf bank_mask:0xf bound_ctrl:0\n" \
  "v_sub_u32 v10, v8, v11\n" \
  "v_bitop3_b32 %1, v9, v8, v10 bitop3:0xb4\n" \
  "v_bitop3_b32 %0, %0, v8, v10 bitop3:0xb4\n" \
  "v_bitop3_b32 v13, v8, v10, v10 bitop3:0x30\n" \
  "v_bfe_u32 v11, %2, 12, 1\n" \
  "v_and_b32_dpp v8, %1, %0 quad_perm:[1,0,3,2] row_mask:0xf bank_mask:0xf bound_ctrl:0\n" \
  "v_mov_b32_dpp v9, %1 quad_perm:[1,0,3,2] row_mask:0xf bank_mask:0xf bound_ctrl:0\n" \
  "v_sub_u32 v10, v8, v11\n" \
  "v_bitop3_b32 %1, v9, v8, v10 bitop3:0xb4\n" \
  "v_bitop3_b32 %0, %0, v8, v10 bitop3:0xb4\n" \
  "v_bitop3_b32 v12, v8, v10, v10 bitop3:0x30\n" \
  "v_bfe_u32 v11, %2, 14, 1\n" \
  "ds_write_b64 %3, v[12:13] offset:3072\n" \
  "v_and_b32_dpp v8, %1, %0 quad_perm:[2,3,0,1] row_mask:0xf bank_mask:0xf bound_ctrl:0\n" \
  "v_mov_b32_dpp v9, %1 quad_perm:[2,3,0,1] row_mask:0xf bank_mask:0xf bound_ctrl:0\n" \
  "v_sub_u32 v10, v8, v11\n" \
  "v_bitop3_b32 %1, v9, v8, v10 bitop3:0xb4\n" \
  "v_bitop3_b32 %0, %0, v8, v10 bitop3:0xb4\n" \
  "v_bitop3_b32 v12, v8, v10, v10 bitop3:0x30\n" \
  "v_bfe_u32 v11, %2, 15, 1\n" \
  "v_and_b32_dpp v8, %1, %0 quad_perm:[1,0,3,2] row_mask:0xf bank_mask:0xf bound_ctrl:0\n" \
  "v_mov_b32_dpp v9, %1 quad_perm:[1,0,3,2] row_mask:0xf bank_mask:0xf bound_ctrl:0\n" \
  "v_sub_u32 v10, v8, v11\n" \
  "v_bitop3_b32 %1, v9, v8, v10 bitop3:0xb4\n" \
  "v_bitop3_b32 %0, %0, v8, v10 bitop3:0xb4\n" \
  "v_bitop3_b32 v13, v8, v10, v10 bitop3:0x30\n" \
  "v_bfe_u32 v11, %2, 31, 1\n" \
  "ds_write_b64 %3, v[12:13] offset:3584\n" \
  "v_mov_b32 v9, %1\n" \
  "s_nop 1\n" \
  "v_permlane16_swap_b32 %1, v9\n" \
  "s_nop 1\n" \
  "v_mov_b32_dpp v9, %1 quad_perm:[0,1,2,3] row_mask:0xa bank_mask:0xf\n" \
  "v_and_b32 v8, v9, %0\n" \
  "v_sub_u32 v10, v8, v11\n" \
  "v_bitop3_b32 %1, v9, v8, v10 bitop3:0xb4\n" \
  "v_bitop3_b32 %0, %0, v8, v10 bitop3:0xb4\n" \
  "v_bitop3_b32 v13, v8, v10, v10 bitop3:0x30\n" \
  "v_bfe_u32 v11, %2, 30, 1\n" \
  "v_and_b32_dpp v8, %1, %0 quad_perm:[1,0,3,2] row_mask:0xf bank_mask:0xf bound_ctrl:0\n" \
  "v_mov_b32_dpp v9, %1 quad_perm:[1,0,3,2] row_mask:0xf bank_mask:0xf bound_ctrl:0\n" \
  "v_sub_u32 v10, v8, v11\n" \
  "v_bitop3_b32 %1, v9, v8, v10 bitop3:0xb4\n" \
  "v_bitop3_b32 %0, %0, v8, v10 bitop3:0xb4\n" \
  "v_bitop3_b32 v12, v8, v10, v10 bitop3:0x30\n" \
  "v_bfe_u32 v11, %2, 28, 1\n" \
  "ds_write_b64 %3, v[12:13] offset:7680\n" \
  "v_and_b32_dpp v8, %1, %0 quad_perm:[2,3,0,1] row_mask:0xf bank_mask:0xf bound_ctrl:0\n" \
  "v_mov_b32_dpp v9, %1 quad_perm:[2,3,0,1] row_mask:0xf bank_mask:0xf bound_ctrl:0\n" \
  "v_sub_u32 v10, v8, v11\n" \
  "v_bitop3_b32 %1, v9, v8, v10 bitop3:0xb4\n" \
  "v_bitop3_b32 %0, %0, v8, v10 bitop3:0xb4\n" \
  "v_bitop3_b32 v12, v8, v10, v10 bitop3:0x30\n" \
  "v_bfe_u32 v11, %2, 29, 1\n" \
  "v_and_b32_dpp v8, %1, %0 quad_perm:[1,0,3,2] row_mask:0xf bank_mask:0xf bound_ctrl:0\n" \
  "v_mov_b32_dpp v9, %1 quad_perm:[1,0,3,2] row_mask:0xf bank_mask:0xf bound_ctrl:0\n" \
  "v_sub_u32 v10, v8, v11\n" \
  "v_bitop3_b32 %1, v9, v8, v10 bitop3:0xb4\n" \
  "v_bitop3_b32 %0, %0, v8, v10 bitop3:0xb4\n" \
  "v_bitop3_b32 v13, v8, v10, v10 bitop3:0x30\n" \
  "v_bfe_u32 v11, %2, 26, 1\n" \
  "ds_write_b64 %3, v[12:13] offset:7168\n" \
  "v_and_b32_dpp v8, %1, %0 row_half_mirror row_mask:0xf bank_mask:0xf bound_ctrl:0\n" \
  "v_mov_b32_dpp v9, %1 row_half_mirror row_mask:0xf bank_mask:0xf bound_ctrl:0\n" \
  "v_sub_u32 v10, v8, v11\n" \
  "v_bitop3_b32 %1, v9, v8, v10 bitop3:0xb4\n" \
  "v_bitop3_b32 %0, %0, v8, v10 bitop3:0xb4\n" \
  "v_bitop3_b32 v12, v8, v10, v10 bitop3:0x30\n" \
  "v_bfe_u32 v11, %2, 27, 1\n" \
  "v_and_b32_dpp v8, %1, %0 quad_perm:[1,0,3,2] row_mask:0xf bank_mask:0xf bound_ctrl:0\n" \
  "v_mov_b32_dpp v9, %1 quad_perm:[1,0,3,2] row_mask:0xf bank_mask:0xf bound_ctrl:0\n" \
  "v_sub_u32 v10, v8, v11\n" \
  "v_bitop3_b32 %1, v9, v8, v10 bitop3:0xb4\n" \
  "v_bitop3_b32 %0, %0, v8, v10 bitop3:0xb4\n" \
  "v_bitop3_b32 v13, v8, v10, v10 bitop3:0x30\n" \
  "v_bfe_u32 v11, %2, 25, 1\n" \
  "ds_write_b64 %3, v[12:13] offset:6656\n" \
  "v_and_b32_dpp v8, %1, %0 quad_perm:[2,3,0,1] row_mask:0xf bank_mask:0xf bound_ctrl:0\n" \
  "v_mov_b32_dpp v9, %1 quad_perm:[2,3,0,1] row_mask:0xf bank_mask:0xf bound_ctrl:0\n" \
  "v_sub_u32 v10, v8, v11\n" \
  "v_bitop3_b32 %1, v9, v8, v10 bitop3:0xb4\n" \
  "v_bitop3_b32 %0, %0, v8, v10 bitop3:0xb4\n" \
  "v_bitop3_b32 v13, v8, v10, v10 bitop3:0x30\n" \
  "v_bfe_u32 v11, %2, 24, 1\n" \
  "v_and_b32_dpp v8, %1, %0 quad_perm:[1,0,3,2] row_mask:0xf bank_mask:0xf bound_ctrl:0\n" \
  "v_mov_b32_dpp v9, %1 quad_perm:[1,0,3,2] row_mask:0xf bank_mask:0xf bound_ctrl:0\n" \
  "v_sub_u32 v10, v8, v11\n" \
  "v_bitop3_b32 %1, v9, v8, v10 bitop3:0xb4\n" \
  "v_bitop3_b32 %0, %0, v8, v10 bitop3:0xb4\n" \
  "v_bitop3_b32 v12, v8, v10, v10 bitop3:0x30\n" \
  "v_bfe_u32 v11, %2, 23, 1\n" \
  "ds_write_b64 %3, v[12:13] offset:6144\n" \
  "v_and_b32_dpp v8, %1, %0 row_mirror row_mask:0xf bank_mask:0xf bound_ctrl:0\n" \
  "v_mov_b32_dpp v9, %1 row_mirror row_mask:0xf bank_mask:0xf bound_ctrl:0\n" \
  "v_sub_u32 v10, v8, v11\n" \
  "v_bitop3_b32 %1, v9, v8, v10 bitop3:0xb4\n" \
  "v_bitop3_b32 %0, %0, v8, v10 bitop3:0xb4\n" \
  "v_bitop3_b32 v13, v8, v10, v10 bitop3:0x30\n" \
  "v_bfe_u32 v11, %2, 22, 1\n" \
  "v_and_b32_dpp v8, %1, %0 quad_perm:[1,0,3,2] row_mask:0xf bank_mask:0xf bound_ctrl:0\n" \
  "v_mov_b32_dpp v9, %1 quad_perm:[1,0,3,2] row_mask:0xf bank_mask:0xf bound_ctrl:0\n" \
  "v_sub_u32 v10, v8, v11\n" \
  "v_bitop3_b32 %1, v9, v8, v10 bitop3:0xb4\n" \
  "v_bitop3_b32 %0, %0, v8, v10 bitop3:0xb4\n" \
  "v_bitop3_b32 v12, v8, v10, v10 bitop3:0x30\n" \
  "v_bfe_u32 v11, %2, 20, 1\n" \
  "ds_write_b64 %3, v[12:13] offset:5632\n" \
  "v_and_b32_dpp v8, %1, %0 quad_perm:[2,3,0,1] row_mask:0xf bank_mask:0xf bound_ctrl:0\n" \
  "v_mov_b32_dpp v9, %1 quad_perm:[2,3,0,1] row_mask:0xf bank_mask:0xf bound_ctrl:0\n" \
  "v_sub_u32 v10, v8, v11\n" \
  "v_bitop3_b32 %1, v9, v8, v10 bitop3:0xb4\n" \
  "v_bitop3_b32 %0, %0, v8, v10 bitop3:0xb4\n" \
  "v_bitop3_b32 v12, v8, v10, v10 bitop3:0x30\n" \
  "v_bfe_u32 v11, %2, 21, 1\n" \
  "v_and_b32_dpp v8, %1, %0 quad_perm:[1,0,3,2] row_mask:0xf bank_mask:0xf bound_ctrl:0\n" \
  "v_mov_b32_dpp v9, %1 quad_perm:[1,0,3,2] row_mask:0xf bank_mask:0xf bound_ctrl:0\n" \
  "v_sub_u32 v10, v8, v11\n" \
  "v_bitop3_b32 %1, v9, v8, v10 bitop3:0xb4\n" \
  "v_bitop3_b32 %0, %0, v8, v10 bitop3:0xb4\n" \
  "v_bitop3_b32 v13, v8, v10, v10 bitop3:0x30\n" \
  "v_bfe_u32 v11, %2, 18, 1\n" \
  "ds_write_b64 %3, v[12:13] offset:5120\n" \
  "v_and_b32_dpp v8, %1, %0 row_half_mirror row_mask:0xf bank_mask:0xf bound_ctrl:0\n" \
  "v_mov_b32_dpp v9, %1 row_half_mirror row_mask:0xf bank_mask:0xf bound_ctrl:0\n" \
  "v_sub_u32 v10, v8, v11\n" \
  "v_bitop3_b32 %1, v9, v8, v10 bitop3:0xb4\n" \
  "v_bitop3_b32 %0, %0, v8, v10 bitop3:0xb4\n" \
  "v_bitop3_b32 v12, v8, v10, v10 bitop3:0x30\n" \
  "v_bfe_u32 v11, %2, 19, 1\n" \
  "v_and_b32_dpp v8, %1, %0 quad_perm:[1,0,3,2] row_mask:0xf bank_mask:0xf bound_ctrl:0\n" \
  "v_mov_b32_dpp v9, %1 quad_perm:[1,0,3,2] row_mask:0xf bank_mask:0xf bound_ctrl:0\n" \
  "v_sub_u32 v10, v8, v11\n" \
  "v_bitop3_b32 %1, v9, v8, v10 bitop3:0xb4\n" \
  "v_bitop3_b32 %0, %0, v8, v10 bitop3:0xb4\n" \
  "v_bitop3_b32 v13, v8, v10, v10 bitop3:0x30\n" \
  "v_bfe_u32 v11, %2, 17, 1\n" \
  "ds_write_b64 %3, v[12:13] offset:4608\n" \
  "v_and_b32_dpp v8, %1, %0 quad_perm:[2,3,0,1] row_mask:0xf bank_mask:0xf bound_ctrl:0\n" \
  "v_mov_b32_dpp v9, %1 quad_perm:[2,3,0,1] row_mask:0xf bank_mask:0xf bound_ctrl:0\n" \
  "v_sub_u32 v10, v8, v11\n" \
  "v_bitop3_b32 %1, v9, v8, v10 bitop3:0xb4\n" \
  "v_bitop3_b32 %0, %0, v8, v10 bitop3:0xb4\n" \
  "v_bitop3_b32 v13, v8, v10, v10 bitop3:0x30\n" \
  "v_bfe_u32 v11, %2, 16, 1\n" \
  "v_and_b32_dpp v8, %1, %0 quad_perm:[1,0,3,2] row_mask:0xf bank_mask:0xf bound_ctrl:0\n" \
  "v_mov_b32_dpp v9, %1 quad_perm:[1,0,3,2] row_mask:0xf bank_mask:0xf bound_ctrl:0\n" \
  "v_sub_u32 v10, v8, v11\n" \
  "v_bitop3_b32 %1, v9, v8, v10 bitop3:0xb4\n" \
  "v_bitop3_b32 %0, %0, v8, v10 bitop3:0xb4\n" \
  "v_bitop3_b32 v12, v8, v10, v10 bitop3:0x30\n" \
  "ds_write_b64 %3, v[12:13] offset:4096\n" \
  ""

constexpr int H_VEC = H_SYNC + 16;
static_assert(H_VEC + 768 <= TB, "LDS map");
template <class F> DEVI void warm_up(const char* ws, const u16* out, const int q, F&& meanwhile) {
  const __amdgpu_buffer_rsrc_t rr = mkrsrc(ws + WS_RS, 12288);
  v4u p[8];
#pragma unroll
  for (int w = 0; w < 8; ++w) p[w] = load16(rr, (q >> 5) * 4096 + (q & 31) * 16 + w * 512, 0, 0);
  const bool w0 = __builtin_amdgcn_readfirstlane(q) < 64;
  unsigned touch;
  asm volatile("global_load_dword %0, %1, %2" : "=&v"(touch) : "v"(w0 ? umin(q, 32u) * 0x100000 : umin(q - 64, 48u) * 0x10000), "s"(w0 ? ws : (const char*)out) : "memory");
  meanwhile();
  v4f m = {0.f, 0.f, 0.f, 0.f};
#pragma unroll
  for (int w = 0; w < 8; ++w) m += __builtin_bit_cast(v4f, p[w]);
  if (q < 96) *(LAS v2u*)(H_VEC + q * 8) = (v2u){pkbf(m[0], m[1]), pkbf(m[2], m[3])};
  asm volatile("s_waitcnt vmcnt(0)" : "+v"(touch) :: "memory");
}
DEVI void build_lists(char* ws, const u16* out, const int ch, const int slice, const int tid, const int lane, const int wave, const int mvoff, const int msoff, unsigned& meta, int& dur) {
  const __amdgpu_buffer_rsrc_t rh = mkrsrc(ws + WS_LIST, HANDB), ro = mkrsrc(ws + WS_O2 + (size_t)ch * SEGB, SEGB);
  const int swave_ = wave; (void)swave_;
  const char* tsrc = ws + WS_B + slice * SLB + lane * 16;
  v4u e4[2];
  {
    const char* sp = ws + WS_SEG + (size_t)ch * SEGB;
    asm volatile("s_cmp_lt_u32 %5, 2\n\ts_cbranch_scc1 Le%=\n\tglobal_load_dwordx4 %0, %2, %3\n\tglobal_load_dwordx4 %1, %2, %4\nLe%=:"
                 : "=&v"(e4[0]), "=&v"(e4[1]) : "v"((tid - 128) * 16), "s"(sp), "s"(sp + SEGB / 2), "s"(wave) : "memory", "scc");
  }
  auto init = [&]() __attribute__((always_inline)) NOPK {
    if (tid < (L2_OUT - L2_CNT) / 16) *(LAS v4u*)(L2_CNT + tid * 16) = (v4u){0, 0, 0, 0};
    *(LAS v4u*)(L2_OUT + tid * 16) = (v4u){0xFFFEu, 0xFFFEu, 0xFFFEu, 0xFFFEu};
    LBAR();
  };
  if (wave >= 2) {
    dma1k(tsrc + (wave - 2) * 1024, TB + (wave - 2) * 1024);
    dma1k(tsrc + (wave + 12) * 1024, TB + (wave + 12) * 1024);
    init();
  } else {
    const v2u o1c = __builtin_bit_cast(v2u, __builtin_amdgcn_raw_buffer_load_b64(mkrsrc(ws + WS_O1C, G1 * 4), tid * 8, 0, 0));
    warm_up(ws, out, tid, [&]() __attribute__((always_inline)) NOPK {
#pragma unroll
      for (int i = 0; i < 4; ++i) store16(((v4u){~0u, ~0u, ~0u, ~0u}), rh, tid * 16, ch * 8192 + i * 2048, AUX_SC1);
      init();
    });
    if (o1c[0] | o1c[1]) *(LAS unsigned*)(M_OV + 4) = 1;
    dma1k(tsrc + (28 + wave) * 1024, TB + (28 + wave) * 1024);
    dma1k(tsrc + (30 + wave) * 1024, TB + (30 + wave) * 1024);
  }
  asm volatile("s_waitcnt vmcnt(3)" : "+v"(e4[0]) :: "memory");
  STAMPG(1, 1);
  unsigned rk[2][4];
  v4u e1;
  auto rank4 = [&](const v4u ev, unsigned (&r)[4]) __attribute__((always_inline)) NOPK {
#pragma unroll
    for (int i = 0; i < 4; ++i) r[i] = LDS_ADD(L2_CNT + (ev[i] >> 16) * 4, 1u);
  };
  const bool full = dppu<0xFF>(e4[0][3]) != ~0u;
  const u64 fullm = wave >= 2 ? __builtin_amdgcn_ballot_w64(full) : 0ull;
  const bool more = fullm != 0;
  if (wave >= 2) rank4(e4[0], rk[0]);
  asm volatile("s_cmp_eq_u64 %1, 0\n\ts_cbranch_scc1 Lm%=\n\ts_waitcnt vmcnt(2)\nLm%=:" : "+v"(e4[1]) : "s"(fullm) : "memory", "scc");
  if (more) {
    e1 = full ? e4[1] : (v4u){~0u, ~0u, ~0u, ~0u};
    rank4(e1, rk[1]);
  }
  LBAR();
  STAMPG(1, 2);
  {
    const int d = 2 * wave;
    const v2u nn = *(LAS v2u*)(L2_CNT + tid * 8);
    const unsigned th = (0x010101u >> (24 - 8 * umin(nn[0], 3u))) | (0x020202u >> (24 - 8 * umin(nn[1], 3u)));
    (void)__hip_atomic_fetch_or((LAS u64*)(L2_MSK + lane * 8), (u64)((th & 3u) << d) | ((u64)(((th >> 8) & 3u) << d) << 32), __ATOMIC_RELAXED, __HIP_MEMORY_SCOPE_WORKGROUP);
    LDS_OR(M_E2 + lane * 4, (th >> 16) << d);
    LDS_OR(M_BK + (lane ^ d) * 4, ((th & 1u) | (dppu<DPP_X1>(th) & 2u)) << d);
    (void)LDS_ADD(M_DG + lane * 4, nn[0] + nn[1]);
  }
  LBAR();
  STAMPG(1, 3);
  const unsigned tcol = now();
  if (wave < 2) {
    __builtin_amdgcn_s_setprio(3);
    unsigned lf, bf, ex;
    if (wave == 0) { const v2u t = *(LAS v2u*)(L2_MSK + lane * 8); lf = ~t[0]; ex = t[1]; bf = ~*(LAS unsigned*)(M_BK + lane * 4); }
    else {
      unsigned dg = *(LAS unsigned*)(M_DG + lane * 4);
      ex = *(LAS unsigned*)(M_E2 + lane * 4);
      dg = umax(dg, dppu<DPP_X1>(dg)); dg = umax(dg, dppu<DPP_X2>(dg)); dg = umax(dg, dppu<DPP_HM>(dg)); dg = umax(dg, dppu<DPP_RM>(dg));
      const unsigned m = umax(umax((unsigned)__builtin_amdgcn_readlane(dg, 0), (unsigned)__builtin_amdgcn_readlane(dg, 16)), umax((unsigned)__builtin_amdgcn_readlane(dg, 32), (unsigned)__builtin_amdgcn_readlane(dg, 48)));
      const unsigned L = umin(umax((m + LGRAN - 1) & ~(LGRAN - 1u), 32u), 64u);
      if (lane == 0) { *(LAS unsigned*)(M_OV + 8) = L; __builtin_amdgcn_raw_buffer_store_b64(((v2u){L >> 1, tcol}), rh, H_META + ch * 128, 0, AUX_SC1); }
      lf = bf = (unsigned)((1ull << (L - 32)) - 1ull);
    }
    asm volatile(COLOUR_ASM : "+v"(lf), "+v"(bf) : "v"(ex), "v"(L2_PICK + wave * 8192 + lane * 8) : "v8", "v9", "v10", "v11", "v12", "v13", "memory");
    *(LAS unsigned*)(M_FR + lane * 8 + wave * 4) = lf;
    STAMPG(1, 10);
    __builtin_amdgcn_s_setprio(0);
  } else {
#pragma unroll
    for (int i = 0; i < 3; ++i) dma1k(tsrc + (30 + wave + 14 * i) * 1024, TB + (30 + wave + 14 * i) * 1024);
    auto place4 = [&](const v4u ev, const unsigned (&r)[4]) __attribute__((always_inline)) NOPK {
      unsigned off[4], tot = 0;
#pragma unroll
      for (int i = 0; i < 4; ++i) {
        const u64 m = __builtin_amdgcn_ballot_w64(r[i] != 0);
        off[i] = __builtin_amdgcn_mbcnt_hi((unsigned)(m >> 32), __builtin_amdgcn_mbcnt_lo((unsigned)m, tot));
        tot += __builtin_popcountll(m);
      }
      unsigned base;
      asm volatile("ds_add_rtn_u32 %0, %1, %2" : "=&v"(base) : "v"(lane == 0 ? M_OV + 12 : LDS_NONE), "v"(tot) : "memory");
#pragma unroll
      for (int i = 0; i < 4; ++i) *(LAS u16*)(L2_OUT + (ev[i] >> 16) * 4 + (umin(r[i], 1u) << 18)) = (u16)ev[i];
      asm volatile("s_waitcnt lgkmcnt(0)" : "+v"(base) :: "memory");
      base = __builtin_amdgcn_readfirstlane(base);
      if (!RARE(base + tot > EXTCAP)) {
#pragma unroll
        for (int i = 0; i < 4; ++i) *(LAS unsigned*)(r[i] ? L2_EXT + (base + off[i]) * 4 : LDS_NONE) = ev[i] | (umin(r[i], 3u) << 27);
      } else {
#pragma unroll
        for (int i = 0; i < 4; ++i)
          if (r[i]) {
            if (base + off[i] < EXTCAP) *(LAS unsigned*)(L2_EXT + (base + off[i]) * 4) = ev[i] | (umin(r[i], 3u) << 27);
            else __builtin_amdgcn_raw_buffer_store_b32(ev[i], ro, LDS_ADD(M_OV, 1u) * 4, 0, AUX_SC1);
          }
      }
    };
    STAMPG(1, 11);
    place4(e4[0], rk[0]); if (more) place4(e1, rk[1]);
    STAMPG(1, 12);
    dma1k(tsrc + (72 + wave) * 1024, TB + (72 + wave) * 1024);
    if (wave < 14) dma1k(tsrc + (86 + wave) * 1024, TB + (86 + wave) * 1024);
  }
  LBAR();
  STAMPG(1, 4);
  meta = __builtin_amdgcn_raw_buffer_load_b32(rh, mvoff, msoff, AUX_SC1);
  const unsigned nex = umin(*(LAS unsigned*)(M_OV + 12), EXTCAP);
  for (unsigned i = tid; i < nex; i += 1024) {
    const unsigned e = *(LAS unsigned*)(L2_EXT + i * 4), r = e >> 27, l8 = (e >> 14) & 0x1F8;
    const unsigned m = *(LAS unsigned*)(r < 3 ? L2_PICK - 8192 + (e >> 16) * 4 : LDS_NONE);
    int c;
    if (m) c = __builtin_ctz(m) + 32 * r - 32;
    else c = kth_high(*(LAS v2u*)(M_FR + l8), LDS_ADD(M_LO + (l8 >> 1), 1u));
    if (!RARE(c < 0)) *(LAS u16*)(L2_OUT + (c >> 1) * 512 + l8 + (c & 1) * 4) = (u16)e;
    else __builtin_amdgcn_raw_buffer_store_b32(e, ro, LDS_ADD(M_OV, 1u) * 4, 0, AUX_SC1);
  }
  WAITVM0();
  asm volatile("" :: "v"(e4[1]));
  LBAR();
  STAMPG(1, 5);
  const unsigned L = *(LAS unsigned*)(M_OV + 8);
  if (8 * wave < L) {
    v4u o;
#pragma unroll
    for (int i = 0; i < 4; ++i) { const v2u t = *(LAS v2u*)(L2_OUT + (wave * 4 + i) * 512 + lane * 8); o[i] = t[0] | (t[1] << 16); }
    store16(o, rh, tid * 16, ch * 8192, AUX_SC1);
  }
  if (tid == 1023) __builtin_amdgcn_raw_buffer_store_b64(*(LAS v2u*)(M_OV), rh, H_META + ch * 128 + 64, 0, AUX_SC1);
  dur = tdiff(now(), tcol);
}

#define GATHER_ASM \
  "s_waitcnt lgkmcnt(0)\n" \
  "v_lshlrev_b32_sdwa v8, 3, %4 dst_sel:DWORD dst_unused:UNUSED_PAD src0_sel:DWORD src1_sel:WORD_0\n" \
  "v_lshlrev_b32_sdwa v10, 3, %4 dst_sel:DWORD dst_unused:UNUSED_PAD src0_sel:DWORD src1_sel:WORD_1\n" \
  "ds_read_b64 v[8:9], v8 offset:61440\n" \
  "ds_read_b64 v[10:11], v10 offset:61440\n" \
  "v_lshlrev_b32_sdwa v12, 3, %5 dst_sel:DWORD dst_unused:UNUSED_PAD src0_sel:DWORD src1_sel:WORD_0\n" \
  "v_lshlrev_b32_sdwa v14, 3, %5 dst_sel:DWORD dst_unused:UNUSED_PAD src0_sel:DWORD src1_sel:WORD_1\n" \
  "ds_read_b64 v[12:13], v12 offset:61440\n" \
  "ds_read_b64 v[14:15], v14 offset:61440\n" \
  "v_lshlrev_b32_sdwa v16, 3, %6 dst_sel:DWORD dst_unused:UNUSED_PAD src0_sel:DWORD src1_sel:WORD_0\n" \
  "v_lshlrev_b32_sdwa v18, 3, %6 dst_sel:DWORD dst_unused:UNUSED_PAD src0_sel:DWORD src1_sel:WORD_1\n" \
  "ds_read_b64 v[16:17], v16 offset:61440\n" \
  "ds_read_b64 v[18:19], v18 offset:61440\n" \
  "v_lshlrev_b32_sdwa v20, 3, %7 dst_sel:DWORD dst_unused:UNUSED_PAD src0_sel:DWORD src1_sel:WORD_0\n" \
  "v_lshlrev_b32_sdwa v22, 3, %7 dst_sel:DWORD dst_unused:UNUSED_PAD src0_sel:DWORD src1_sel:WORD_1\n" \
  "ds_read_b64 v[20:21], v20 offset:61440\n" \
  "ds_read_b64 v[22:23], v22 offset:61440\n" \
  "s_waitcnt lgkmcnt(4)\n" \
  "v_smfmac_f32_16x16x64_bf16 %0, %1, v[8:15], %2\n" \
  "v_lshlrev_b32_sdwa v8, 3, %8 dst_sel:DWORD dst_unused:UNUSED_PAD src0_sel:DWORD src1_sel:WORD_0\n" \
  "v_lshlrev_b32_sdwa v10, 3, %8 dst_sel:DWORD dst_unused:UNUSED_PAD src0_sel:DWORD src1_sel:WORD_1\n" \
  "ds_read_b64 v[8:9], v8 offset:61440\n" \
  "ds_read_b64 v[10:11], v10 offset:61440\n" \
  "v_lshlrev_b32_sdwa v12, 3, %9 dst_sel:DWORD dst_unused:UNUSED_PAD src0_sel:DWORD src1_sel:WORD_0\n" \
  "v_lshlrev_b32_sdwa v14, 3, %9 dst_sel:DWORD dst_unused:UNUSED_PAD src0_sel:DWORD src1_sel:WORD_1\n" \
  "ds_read_b64 v[12:13], v12 offset:61440\n" \
  "ds_read_b64 v[14:15], v14 offset:61440\n" \
  "s_waitcnt lgkmcnt(4)\n" \
  "v_smfmac_f32_16x16x64_bf16 %0, %1, v[16:23], %2\n" \
  "v_lshlrev_b32_sdwa v16, 3, %10 dst_sel:DWORD dst_unused:UNUSED_PAD src0_sel:DWORD src1_sel:WORD_0\n" \
  "v_lshlrev_b32_sdwa v18, 3, %10 dst_sel:DWORD dst_unused:UNUSED_PAD src0_sel:DWORD src1_sel:WORD_1\n" \
  "ds_read_b64 v[16:17], v16 offset:61440\n" \
  "ds_read_b64 v[18:19], v18 offset:61440\n" \
  "v_lshlrev_b32_sdwa v20, 3, %11 dst_sel:DWORD dst_unused:UNUSED_PAD src0_sel:DWORD src1_sel:WORD_0\n" \
  "v_lshlrev_b32_sdwa v22, 3, %11 dst_sel:DWORD dst_unused:UNUSED_PAD src0_sel:DWORD src1_sel:WORD_1\n" \
  "ds_read_b64 v[20:21], v20 offset:61440\n" \
  "ds_read_b64 v[22:23], v22 offset:61440\n" \
  "s_waitcnt lgkmcnt(4)\n" \
  "v_smfmac_f32_16x16x64_bf16 %0, %1, v[8:15], %2\n" \
  "v_lshlrev_b32_sdwa v8, 3, %12 dst_sel:DWORD dst_unused:UNUSED_PAD src0_sel:DWORD src1_sel:WORD_0\n" \
  "v_lshlrev_b32_sdwa v10, 3, %12 dst_sel:DWORD dst_unused:UNUSED_PAD src0_sel:DWORD src1_sel:WORD_1\n" \
  "ds_read_b64 v[8:9], v8 offset:61440\n" \
  "ds_read_b64 v[10:11], v10 offset:61440\n" \
  "v_lshlrev_b32_sdwa v12, 3, %13 dst_sel:DWORD dst_unused:UNUSED_PAD src0_sel:DWORD src1_sel:WORD_0\n" \
  "v_lshlrev_b32_sdwa v14, 3, %13 dst_sel:DWORD dst_unused:UNUSED_PAD src0_sel:DWORD src1_sel:WORD_1\n" \
  "ds_read_b64 v[12:13], v12 offset:61440\n" \
  "ds_read_b64 v[14:15], v14 offset:61440\n" \
  "s_waitcnt lgkmcnt(4)\n" \
  "v_smfmac_f32_16x16x64_bf16 %0, %1, v[16:23], %2\n" \
  "v_lshlrev_b32_sdwa v16, 3, %14 dst_sel:DWORD dst_unused:UNUSED_PAD src0_sel:DWORD src1_sel:WORD_0\n" \
  "v_lshlrev_b32_sdwa v18, 3, %14 dst_sel:DWORD dst_unused:UNUSED_PAD src0_sel:DWORD src1_sel:WORD_1\n" \
  "ds_read_b64 v[16:17], v16 offset:61440\n" \
  "ds_read_b64 v[18:19], v18 offset:61440\n" \
  "v_lshlrev_b32_sdwa v20, 3, %15 dst_sel:DWORD dst_unused:UNUSED_PAD src0_sel:DWORD src1_sel:WORD_0\n" \
  "v_lshlrev_b32_sdwa v22, 3, %15 dst_sel:DWORD dst_unused:UNUSED_PAD src0_sel:DWORD src1_sel:WORD_1\n" \
  "ds_read_b64 v[20:21], v20 offset:61440\n" \
  "ds_read_b64 v[22:23], v22 offset:61440\n" \
  "s_waitcnt lgkmcnt(4)\n" \
  "v_smfmac_f32_16x16x64_bf16 %0, %1, v[8:15], %2\n" \
  "v_lshlrev_b32_sdwa v8, 3, %16 dst_sel:DWORD dst_unused:UNUSED_PAD src0_sel:DWORD src1_sel:WORD_0\n" \
  "v_lshlrev_b32_sdwa v10, 3, %16 dst_sel:DWORD dst_unused:UNUSED_PAD src0_sel:DWORD src1_sel:WORD_1\n" \
  "ds_read_b64 v[8:9], v8 offset:61440\n" \
  "ds_read_b64 v[10:11], v10 offset:61440\n" \
  "v_lshlrev_b32_sdwa v12, 3, %17 dst_sel:DWORD dst_unused:UNUSED_PAD src0_sel:DWORD src1_sel:WORD_0\n" \
  "v_lshlrev_b32_sdwa v14, 3, %17 dst_sel:DWORD dst_unused:UNUSED_PAD src0_sel:DWORD src1_sel:WORD_1\n" \
  "ds_read_b64 v[12:13], v12 offset:61440\n" \
  "ds_read_b64 v[14:15], v14 offset:61440\n" \
  "s_waitcnt lgkmcnt(4)\n" \
  "v_smfmac_f32_16x16x64_bf16 %0, %1, v[16:23], %2\n" \
  "v_lshlrev_b32_sdwa v16, 3, %18 dst_sel:DWORD dst_unused:UNUSED_PAD src0_sel:DWORD src1_sel:WORD_0\n" \
  "v_lshlrev_b32_sdwa v18, 3, %18 dst_sel:DWORD dst_unused:UNUSED_PAD src0_sel:DWORD src1_sel:WORD_1\n" \
  "ds_read_b64 v[16:17], v16 offset:61440\n" \
  "ds_read_b64 v[18:19], v18 offset:61440\n" \
  "v_lshlrev_b32_sdwa v20, 3, %19 dst_sel:DWORD dst_unused:UNUSED_PAD src0_sel:DWORD src1_sel:WORD_0\n" \
  "v_lshlrev_b32_sdwa v22, 3, %19 dst_sel:DWORD dst_unused:UNUSED_PAD src0_sel:DWORD src1_sel:WORD_1\n" \
  "ds_read_b64 v[20:21], v20 offset:61440\n" \
  "ds_read_b64 v[22:23], v22 offset:61440\n" \
  "s_cmp_le_u32 %3, 16\n" \
  "s_cbranch_scc1 Lgt%=\n" \
  "s_waitcnt lgkmcnt(4)\n" \
  "v_smfmac_f32_16x16x64_bf16 %0, %1, v[8:15], %2\n" \
  "v_lshlrev_b32_sdwa v8, 3, %20 dst_sel:DWORD dst_unused:UNUSED_PAD src0_sel:DWORD src1_sel:WORD_0\n" \
  "v_lshlrev_b32_sdwa v10, 3, %20 dst_sel:DWORD dst_unused:UNUSED_PAD src0_sel:DWORD src1_sel:WORD_1\n" \
  "ds_read_b64 v[8:9], v8 offset:61440\n" \
  "ds_read_b64 v[10:11], v10 offset:61440\n" \
  "v_lshlrev_b32_sdwa v12, 3, %21 dst_sel:DWORD dst_unused:UNUSED_PAD src0_sel:DWORD src1_sel:WORD_0\n" \
  "v_lshlrev_b32_sdwa v14, 3, %21 dst_sel:DWORD dst_unused:UNUSED_PAD src0_sel:DWORD src1_sel:WORD_1\n" \
  "ds_read_b64 v[12:13], v12 offset:61440\n" \
  "ds_read_b64 v[14:15], v14 offset:61440\n" \
  "s_cmp_le_u32 %3, 18\n" \
  "s_cbranch_scc1 Lgt%=\n" \
  "s_waitcnt lgkmcnt(4)\n" \
  "v_smfmac_f32_16x16x64_bf16 %0, %1, v[16:23], %2\n" \
  "v_lshlrev_b32_sdwa v16, 3, %22 dst_sel:DWORD dst_unused:UNUSED_PAD src0_sel:DWORD src1_sel:WORD_0\n" \
  "v_lshlrev_b32_sdwa v18, 3, %22 dst_sel:DWORD dst_unused:UNUSED_PAD src0_sel:DWORD src1_sel:WORD_1\n" \
  "ds_read_b64 v[16:17], v16 offset:61440\n" \
  "ds_read_b64 v[18:19], v18 offset:61440\n" \
  "v_lshlrev_b32_sdwa v20, 3, %23 dst_sel:DWORD dst_unused:UNUSED_PAD src0_sel:DWORD src1_sel:WORD_0\n" \
  "v_lshlrev_b32_sdwa v22, 3, %23 dst_sel:DWORD dst_unused:UNUSED_PAD src0_sel:DWORD src1_sel:WORD_1\n" \
  "ds_read_b64 v[20:21], v20 offset:61440\n" \
  "ds_read_b64 v[22:23], v22 offset:61440\n" \
  "s_cmp_le_u32 %3, 20\n" \
  "s_cbranch_scc1 Lgt%=\n" \
  "s_waitcnt lgkmcnt(4)\n" \
  "v_smfmac_f32_16x16x64_bf16 %0, %1, v[8:15], %2\n" \
  "v_lshlrev_b32_sdwa v8, 3, %24 dst_sel:DWORD dst_unused:UNUSED_PAD src0_sel:DWORD src1_sel:WORD_0\n" \
  "v_lshlrev_b32_sdwa v10, 3, %24 dst_sel:DWORD dst_unused:UNUSED_PAD src0_sel:DWORD src1_sel:WORD_1\n" \
  "ds_read_b64 v[8:9], v8 offset:61440\n" \
  "ds_read_b64 v[10:11], v10 offset:61440\n" \
  "v_lshlrev_b32_sdwa v12, 3, %25 dst_sel:DWORD dst_unused:UNUSED_PAD src0_sel:DWORD src1_sel:WORD_0\n" \
  "v_lshlrev_b32_sdwa v14, 3, %25 dst_sel:DWORD dst_unused:UNUSED_PAD src0_sel:DWORD src1_sel:WORD_1\n" \
  "ds_read_b64 v[12:13], v12 offset:61440\n" \
  "ds_read_b64 v[14:15], v14 offset:61440\n" \
  "s_cmp_le_u32 %3, 22\n" \
  "s_cbranch_scc1 Lgt%=\n" \
  "s_waitcnt lgkmcnt(4)\n" \
  "v_smfmac_f32_16x16x64_bf16 %0, %1, v[16:23], %2\n" \
  "v_lshlrev_b32_sdwa v16, 3, %26 dst_sel:DWORD dst_unused:UNUSED_PAD src0_sel:DWORD src1_sel:WORD_0\n" \
  "v_lshlrev_b32_sdwa v18, 3, %26 dst_sel:DWORD dst_unused:UNUSED_PAD src0_sel:DWORD src1_sel:WORD_1\n" \
  "ds_read_b64 v[16:17], v16 offset:61440\n" \
  "ds_read_b64 v[18:19], v18 offset:61440\n" \
  "v_lshlrev_b32_sdwa v20, 3, %27 dst_sel:DWORD dst_unused:UNUSED_PAD src0_sel:DWORD src1_sel:WORD_0\n" \
  "v_lshlrev_b32_sdwa v22, 3, %27 dst_sel:DWORD dst_unused:UNUSED_PAD src0_sel:DWORD src1_sel:WORD_1\n" \
  "ds_read_b64 v[20:21], v20 offset:61440\n" \
  "ds_read_b64 v[22:23], v22 offset:61440\n" \
  "s_cmp_le_u32 %3, 24\n" \
  "s_cbranch_scc1 Lgt%=\n" \
  "s_waitcnt lgkmcnt(4)\n" \
  "v_smfmac_f32_16x16x64_bf16 %0, %1, v[8:15], %2\n" \
  "v_lshlrev_b32_sdwa v8, 3, %28 dst_sel:DWORD dst_unused:UNUSED_PAD src0_sel:DWORD src1_sel:WORD_0\n" \
  "v_lshlrev_b32_sdwa v10, 3, %28 dst_sel:DWORD dst_unused:UNUSED_PAD src0_sel:DWORD src1_sel:WORD_1\n" \
  "ds_read_b64 v[8:9], v8 offset:61440\n" \
  "ds_read_b64 v[10:11], v10 offset:61440\n" \
  "v_lshlrev_b32_sdwa v12, 3, %29 dst_sel:DWORD dst_unused:UNUSED_PAD src0_sel:DWORD src1_sel:WORD_0\n" \
  "v_lshlrev_b32_sdwa v14, 3, %29 dst_sel:DWORD dst_unused:UNUSED_PAD src0_sel:DWORD src1_sel:WORD_1\n" \
  "ds_read_b64 v[12:13], v12 offset:61440\n" \
  "ds_read_b64 v[14:15], v14 offset:61440\n" \
  "s_cmp_le_u32 %3, 26\n" \
  "s_cbranch_scc1 Lgt%=\n" \
  "s_waitcnt lgkmcnt(4)\n" \
  "v_smfmac_f32_16x16x64_bf16 %0, %1, v[16:23], %2\n" \
  "v_lshlrev_b32_sdwa v16, 3, %30 dst_sel:DWORD dst_unused:UNUSED_PAD src0_sel:DWORD src1_sel:WORD_0\n" \
  "v_lshlrev_b32_sdwa v18, 3, %30 dst_sel:DWORD dst_unused:UNUSED_PAD src0_sel:DWORD src1_sel:WORD_1\n" \
  "ds_read_b64 v[16:17], v16 offset:61440\n" \
  "ds_read_b64 v[18:19], v18 offset:61440\n" \
  "v_lshlrev_b32_sdwa v20, 3, %31 dst_sel:DWORD dst_unused:UNUSED_PAD src0_sel:DWORD src1_sel:WORD_0\n" \
  "v_lshlrev_b32_sdwa v22, 3, %31 dst_sel:DWORD dst_unused:UNUSED_PAD src0_sel:DWORD src1_sel:WORD_1\n" \
  "ds_read_b64 v[20:21], v20 offset:61440\n" \
  "ds_read_b64 v[22:23], v22 offset:61440\n" \
  "s_cmp_le_u32 %3, 28\n" \
  "s_cbranch_scc1 Lgt%=\n" \
  "s_waitcnt lgkmcnt(4)\n" \
  "v_smfmac_f32_16x16x64_bf16 %0, %1, v[8:15], %2\n" \
  "v_lshlrev_b32_sdwa v8, 3, %32 dst_sel:DWORD dst_unused:UNUSED_PAD src0_sel:DWORD src1_sel:WORD_0\n" \
  "v_lshlrev_b32_sdwa v10, 3, %32 dst_sel:DWORD dst_unused:UNUSED_PAD src0_sel:DWORD src1_sel:WORD_1\n" \
  "ds_read_b64 v[8:9], v8 offset:61440\n" \
  "ds_read_b64 v[10:11], v10 offset:61440\n" \
  "v_lshlrev_b32_sdwa v12, 3, %33 dst_sel:DWORD dst_unused:UNUSED_PAD src0_sel:DWORD src1_sel:WORD_0\n" \
  "v_lshlrev_b32_sdwa v14, 3, %33 dst_sel:DWORD dst_unused:UNUSED_PAD src0_sel:DWORD src1_sel:WORD_1\n" \
  "ds_read_b64 v[12:13], v12 offset:61440\n" \
  "ds_read_b64 v[14:15], v14 offset:61440\n" \
  "s_cmp_le_u32 %3, 30\n" \
  "s_cbranch_scc1 Lgt%=\n" \
  "s_waitcnt lgkmcnt(4)\n" \
  "v_smfmac_f32_16x16x64_bf16 %0, %1, v[16:23], %2\n" \
  "v_lshlrev_b32_sdwa v16, 3, %34 dst_sel:DWORD dst_unused:UNUSED_PAD src0_sel:DWORD src1_sel:WORD_0\n" \
  "v_lshlrev_b32_sdwa v18, 3, %34 dst_sel:DWORD dst_unused:UNUSED_PAD src0_sel:DWORD src1_sel:WORD_1\n" \
  "ds_read_b64 v[16:17], v16 offset:61440\n" \
  "ds_read_b64 v[18:19], v18 offset:61440\n" \
  "v_lshlrev_b32_sdwa v20, 3, %35 dst_sel:DWORD dst_unused:UNUSED_PAD src0_sel:DWORD src1_sel:WORD_0\n" \
  "v_lshlrev_b32_sdwa v22, 3, %35 dst_sel:DWORD dst_unused:UNUSED_PAD src0_sel:DWORD src1_sel:WORD_1\n" \
  "ds_read_b64 v[20:21], v20 offset:61440\n" \
  "ds_read_b64 v[22:23], v22 offset:61440\n" \
  "Lgt%=:\n" \
  "s_waitcnt lgkmcnt(0)\n" \
  "v_smfmac_f32_16x16x64_bf16 %0, %1, v[8:15], %2\n" \
  "v_smfmac_f32_16x16x64_bf16 %0, %1, v[16:23], %2\n" \
  "s_nop 7\n" \
  ""
#define GATHER_CLOB "v8", "v9", "v10", "v11", "v12", "v13", "v14", "v15", "v16", "v17", "v18", "v19", "v20", "v21", "v22", "v23", "scc"

DEVI v4f gather(const v4u (&L)[8], int n, v8bf sel, int sidx) {
  v4f acc = {0.f, 0.f, 0.f, 0.f};
  asm volatile(GATHER_ASM : "+v"(acc) : "v"(sel), "v"(sidx), "s"(n),
    "v"(L[0][0]), "v"(L[0][1]), "v"(L[0][2]), "v"(L[0][3]), "v"(L[1][0]), "v"(L[1][1]), "v"(L[1][2]), "v"(L[1][3]),
    "v"(L[2][0]), "v"(L[2][1]), "v"(L[2][2]), "v"(L[2][3]), "v"(L[3][0]), "v"(L[3][1]), "v"(L[3][2]), "v"(L[3][3]),
    "v"(L[4][0]), "v"(L[4][1]), "v"(L[4][2]), "v"(L[4][3]), "v"(L[5][0]), "v"(L[5][1]), "v"(L[5][2]), "v"(L[5][3]),
    "v"(L[6][0]), "v"(L[6][1]), "v"(L[6][2]), "v"(L[6][3]), "v"(L[7][0]), "v"(L[7][1]), "v"(L[7][2]), "v"(L[7][3])
    : GATHER_CLOB, "memory");
  return acc;
}
DEVI v4f slow_edges(v4f acc, const char* ws, int ch, int n2, int any1, int lane) {
  const __amdgpu_buffer_rsrc_t ro = mkrsrc(ws + WS_O2 + (size_t)ch * SEGB, SEGB);
  const int lists = any1 ? G1 : 0;
  for (int li = -1; li < lists; ++li) {
    const int cnt = li < 0 ? n2 : (int)((const unsigned*)(ws + WS_O1C))[li];
    for (int base = 0; base < cnt; base += 64) {
      bool ok = base + lane < cnt;
      unsigned tl = 0, p = 0;
      if (ok) {
        if (li < 0) { const unsigned e = __builtin_amdgcn_raw_buffer_load_b32(ro, (base + lane) * 4, 0, AUX_SC1); tl = (e >> 17) & 63; p = e & 0xFFFF; }
        else { const unsigned e = ((const unsigned*)(ws + WS_O1))[li * 2048 + base + lane]; ok = (int)(e >> 20) == ch; tl = (e >> 14) & 63; p = e & 0x3FFF; }
      }
      const v2u val = *(LAS v2u*)(TB + p * 8);
      u64 m = __builtin_amdgcn_ballot_w64(ok);
      while (m) {
        const int i = __builtin_ctzll(m); m &= m - 1;
        const unsigned t = __builtin_amdgcn_readlane(tl, i), a = __builtin_amdgcn_readlane(val[0], i), c = __builtin_amdgcn_readlane(val[1], i);
        if ((unsigned)lane == t) { acc[0] += bflo(a); acc[1] += bfhi(a); acc[2] += bflo(c); acc[3] += bfhi(c); }
      }
    }
  }
  return acc;
}
DEVI unsigned pksafe(float a, float b) { return umin(pkbf(a, b), 0xFFFFFFFEu); }
DEVI v2u publish(v4f a, float d2, __amdgpu_buffer_rsrc_t rs, int soff, int lane) {
  const unsigned x0 = pksafe(a[0] * d2, a[1] * d2), x1 = pksafe(a[2] * d2, a[3] * d2);
  const unsigned y0 = dppu<0xF5>(x0), y1 = dppu<0xF5>(x1);
  if (!(lane & 1)) store16(((v4u){x0, x1, y0, y1}), rs, lane * 8, soff, AUX_SC1);
  return (v2u){x0, x1};
}
DEVI void issue_lists(v4u (&L)[8], v2u& ov, const __amdgpu_buffer_rsrc_t rh, const int c, const int n, const int l16) {
  CFENCE();
  ov = __builtin_bit_cast(v2u, __builtin_amdgcn_raw_buffer_load_b64(rh, H_META + 64, c * 128, AUX_SC1));
#pragma unroll
  for (int i = 4; i < 8; ++i) asm volatile("" : "=v"(L[i]));
#pragma unroll
  for (int i = 0; i < 8; ++i)
    if (i < 4 || 4 * i < n) L[i] = load16(rh, l16, c * 8192 + i * 1024, AUX_SC1);
}
DEVI void check_lists(v4u (&L)[8], v2u& ov, const char* ws, const int c, const int n, const int l16) {
  const char* pa = ws + WS_LIST + c * 8192 + 4096;
  const char* pm = ws + WS_META + c * 128;
  unsigned sp = 0;
  bool redo;
#pragma unroll 1
  do {
    asm volatile("s_cmp_eq_u32 %12, 0\n\ts_cbranch_scc1 Lck%=\n\ts_sleep 8\n\t"
                 "global_load_dwordx4 %0, %9, %10 offset:-4096 sc1\n\tglobal_load_dwordx4 %1, %9, %10 offset:-3072 sc1\n\tglobal_load_dwordx4 %2, %9, %10 offset:-2048 sc1\n\t"
                 "global_load_dwordx4 %3, %9, %10 offset:-1024 sc1\n\tglobal_load_dwordx4 %4, %9, %10 sc1\n\tglobal_load_dwordx4 %5, %9, %10 offset:1024 sc1\n\t"
                 "global_load_dwordx4 %6, %9, %10 offset:2048 sc1\n\tglobal_load_dwordx4 %7, %9, %10 offset:3072 sc1\n\tglobal_load_dwordx2 %8, %13, %11 sc1\n\ts_waitcnt vmcnt(0)\n"
                 "Lck%=:"
                 : "+v"(L[0]), "+v"(L[1]), "+v"(L[2]), "+v"(L[3]), "+v"(L[4]), "+v"(L[5]), "+v"(L[6]), "+v"(L[7]), "+v"(ov) : "v"(l16), "s"(pa), "s"(pm), "s"(sp), "v"(64) : "memory", "scc");
    unsigned m = umax(ov[0], ov[1]);
#pragma unroll
    for (int i = 0; i < 8; ++i) m = umax(m, (i < 4 || 4 * i < n) ? max4(L[i]) : 0u);
    redo = anyl(m == ~0u) & (++sp < SPINCAP);
  } while (redo);
}
DEVI unsigned wait_flags(const __amdgpu_buffer_rsrc_t rh, const int voff) {
  unsigned f;
#pragma unroll 1
  for (unsigned sp = 0; sp < SPINCAP; ++sp) {
    CFENCE();
    f = __builtin_amdgcn_raw_buffer_load_b32(rh, voff, H_FLAG, AUX_SC1);
    if (!anyl(f == ~0u)) break;
    __builtin_amdgcn_s_sleep(8);
  }
  CFENCE();
  return f;
}
#define TC1 4
#define TC2 18
#define TCL 170
#define TCW 15
#define TCX 40
#define TC3 28
#define TCH 20
#define TCG 13
#define TC7 22
#define TCW2 20
#define PRA 2
#define PRL 2
#define PRB 0
#define NA1 4
#define XSLEEP2 1
#define TC0 12
#define XSLEEP 1

template <int I, int E, class F> DEVI void sfor(F&& f) { if constexpr (I < E) { f(IC<I>{}); sfor<I + 1, E>(f); } }
template <int RM, int BM> DEVI float dppmerge(float old, float x) { return u2f((unsigned)__builtin_amdgcn_update_dpp((int)f2u(old), (int)f2u(x), 0xE4, RM, BM, false)); }
DEVI int lane_again() { int l; asm volatile("v_mbcnt_lo_u32_b32 %0, -1, 0\n\tv_mbcnt_hi_u32_b32 %0, -1, %0" : "=v"(l)); return l; }
DEVI float bperm(int byteaddr, float v) { return u2f((unsigned)__builtin_amdgcn_ds_bpermute(byteaddr, (int)f2u(v))); }
DEVI float vmax(float a, float b) { float r; asm("v_max_f32 %0, %1, %2" : "=v"(r) : "v"(a), "v"(b)); return r; }
DEVI float vmax32(float x) { v2u r = __builtin_amdgcn_permlane32_swap(f2u(x), f2u(x), false, false); return vmax(u2f(r[0]), u2f(r[1])); }
DEVI float swapadd16(float a, float b) { v2u r = __builtin_amdgcn_permlane16_swap(f2u(a), f2u(b), false, false); return u2f(r[0]) + u2f(r[1]); }
DEVI float swapadd32(float a, float b) { v2u r = __builtin_amdgcn_permlane32_swap(f2u(a), f2u(b), false, false); return u2f(r[0]) + u2f(r[1]); }
DEVI v8bf pack8(const v16f& a, int o) { return __builtin_bit_cast(v8bf, (v4u){pkbf(a[o], a[o + 1]), pkbf(a[o + 2], a[o + 3]), pkbf(a[o + 4], a[o + 5]), pkbf(a[o + 6], a[o + 7])}); }
#define TSTAMP(i) do {} while (0)
#define PQ0 3
#define PQ1 2
#define PQ2 1
#define PF1 24
#define PF2 48
#define OUTAUX 16
#define MFMA16(a, b, c) __builtin_amdgcn_mfma_f32_16x16x32_bf16(a, b, c, 0, 0, 0)
#define MFMA32(a, b, c) __builtin_amdgcn_mfma_f32_32x32x16_bf16(a, b, c, 0, 0, 0)
#define PIN() __builtin_amdgcn_sched_barrier(0x406)
#define TP(i) do {} while (0)
#define LM(c) __builtin_amdgcn_inverse_ballot_w64(c)
DEVI float vmax3(float a, float b, float c) { float r; asm("v_max3_f32 %0, %1, %2, %3" : "=v"(r) : "v"(a), "v"(b), "v"(c)); return r; }
struct TfAddr { int go, xb, wb, sc, key, tr; };
DEVI TfAddr tf_addr(const int bi, const bool half, const int lane) {
  const int h = lane >> 5, n = (lane >> 3) & 3, hop = lane & 7, row = lane & 15, r = row & 3, up = row >> 3, q = (lane >> 2) & 3;
  TfAddr t;
  t.go = half && n > 1 ? 0x70000 : T_TOK + (hop < 4 ? hop : 11 - hop) * T_HS + h * 800 + bi * 32 + n * 8;
  t.xb = (((row >> 2) ^ (lane >> 4)) & 1) || (r == 2 && up) ? 0x7F000 : T_X + (r + (r == 3 ? up : 0)) * 256 + h * 16;
  t.wb = T_W + lane * 16;
  t.sc = T_SCR + bi * 256 + lane * 4;
  t.key = T_SCR + bi * 256 + n * 32 + h * 16;
  t.tr = T_TOK + (h ? 7 - q : q) * T_HS + (((lane >> 4) & 1) * 4 + (lane & 3)) * 400 + bi * 32;
  asm volatile("" : "+v"(t.go), "+v"(t.xb), "+v"(t.wb), "+v"(t.sc), "+v"(t.key), "+v"(t.tr));
  return t;
}
template <int MODE>
DEVI void tf_batch(const int bi, const TfAddr ad, const v8bf (&mreg)[RF], const float ca, const unsigned nd01, const unsigned nd23, const char* ws, u16* __restrict__ out) {
  constexpr int PD = 3;
  const v16f zero = {0.f, 0.f, 0.f, 0.f, 0.f, 0.f, 0.f, 0.f, 0.f, 0.f, 0.f, 0.f, 0.f, 0.f, 0.f, 0.f};
  LAS char* const wb = (LAS char*)ad.wb;
  LAS char* const xb = (LAS char*)ad.xb;
  LAS char* const scw = (LAS char*)ad.sc;
  LAS char* const scr = (LAS char*)ad.key;
  __builtin_amdgcn_s_setprio(MODE ? 2 : 1);
  const float cc = LM(0x0101010101010101ull) ? 1.f : ca;
  v8bf go[8], w[64], xreg[8];
#pragma unroll
  for (int f = 0; f < RF; ++f)
    if constexpr (MODE != 2) w[f] = mreg[f];
#pragma unroll
  for (int s = 0; s < 8; ++s) {
    const v4bf lo = *(volatile LAS v4bf*)((LAS char*)ad.go + s * 1600), hi = *(volatile LAS v4bf*)((LAS char*)ad.go + s * 1600 + 400);
    go[s] = __builtin_shufflevector(lo, hi, 0, 1, 2, 3, 4, 5, 6, 7);
  }
  TP(0);
  PIN();
  v4f xq = {0.f, 0.f, 0.f, 0.f};
  constexpr int XF0 = 11;
  v4f gr0 = xq;
  v8bf zb[4][2], pa[2];
  v16f acc[8], S;
  v4f rl, ga, gb, sc;
  float rho, t1, t3, sh, wmean;
  unsigned p0, p1;
  constexpr int F0 = MODE == 2 ? 32 : 0, F1 = MODE == 1 ? 32 : 64, NG = MODE ? 1 : 2;
  constexpr u64 N0 = 0x000000FF000000FFull, D0 = 0x0000003000000003ull;
  v8bf da[2], gt[4][2];
  auto mkpa = [&](int i) __attribute__((always_inline)) NOPK {
    pa[i] = __builtin_bit_cast(v8bf, (v4u){LM(N0 << (16 * i)) ? p0 : 0u, LM(N0 << (16 * i)) ? p1 : 0u, LM(N0 << (16 * i + 8)) ? p0 : 0u, LM(N0 << (16 * i + 8)) ? p1 : 0u});
  };
  auto mkda = [&](int i) __attribute__((always_inline)) NOPK {
    const unsigned one = LM(0xAAAAAAAAAAAAAAAAull) ? 0x3F800000u : 0x3F80u;
    da[i] = __builtin_bit_cast(v8bf, (v4u){LM(D0 << (16 * i)) ? one : 0u, LM(D0 << (16 * i + 2)) ? one : 0u, LM(D0 << (16 * i + 8)) ? one : 0u, LM(D0 << (16 * i + 10)) ? one : 0u});
  };
  auto rdgt = [&](int T) __attribute__((always_inline)) NOPK {
#pragma unroll
    for (int sg = 0; sg < NG; ++sg) {
      LAS char* const q = (LAS char*)ad.tr + T * 3200 + sg * 16;
      gt[T][sg] = __builtin_shufflevector(__builtin_amdgcn_ds_read_tr16_b64_v4bf16((LAS v4bf*)q), __builtin_amdgcn_ds_read_tr16_b64_v4bf16((LAS v4bf*)(q + 8)), 0, 1, 2, 3, 4, 5, 6, 7);
    }
  };
  sfor<(MODE == 2 ? 32 - PD : 0), (MODE == 1 ? 53 : 64)>([&](auto fi) __attribute__((always_inline)) NOPK {
    constexpr int f = decltype(fi)::v;
    if constexpr (f == 20 && MODE == 0)
      asm volatile("s_cmp_gt_u32 %2, 3\n\ts_cbranch_scc1 La%=\n\ts_waitcnt vmcnt(0)\n\tds_add_u32 %0, %1\n\ts_waitcnt lgkmcnt(0)\nLa%=:" :: "v"(LM(1ull) ? T_HAND + 132 : LDS_NONE), "v"(1), "s"(bi) : "memory", "scc");
    if constexpr (f + PD == 32 && MODE != 1) {
      unsigned tmp;
      asm volatile("s_mov_b32 %1, %3\nLw%=:\n\tds_read_b32 %0, %2\n\ts_sub_u32 %1, %1, 1\n\ts_cbranch_scc1 Lx%=\n\ts_waitcnt lgkmcnt(0)\n\tv_cmp_ne_u32 vcc, 4, %0\n\ts_cbranch_vccnz Lw%=\nLx%=:\n\ts_waitcnt lgkmcnt(0)"
                   : "=&v"(p0), "=&s"(tmp) : "v"(T_HAND + 132), "i"(SPINCAP) : "memory", "vcc", "scc");
    }
    if constexpr (f + PD >= RF && f + PD < F1) w[f + PD] = *(LAS v8bf*)(wb + (f + PD - RF) * 1024);
    constexpr int T = f >> 3, s = f & 7;
    if constexpr (f >= F0 && f < F1) acc[T] = T < 4 ? MFMA32(w[f], go[s], s == 0 ? zero : acc[T]) : MFMA32(go[s], w[f], s == 0 ? zero : acc[T]);
    if constexpr (T > 0 && (s == 1 || s == 2)) {
      constexpr int U = T - 1, sg = s - 1;
      if constexpr (U < 4) { if constexpr (MODE != 2) S = MFMA32(go[2 * U + sg], pack8(acc[U], 8 * sg), (U == 0 && sg == 0) ? zero : S); }
      else if constexpr (MODE != 1 && sg < NG) { zb[U - 4][sg] = pack8(acc[U], 8 * sg); asm volatile("" : "+v"(zb[U - 4][sg])); }
    }
    if constexpr (MODE != 2 && f >= XF0 - 2 && f < XF0 + 6) xreg[f - XF0 + 2] = *(LAS v8bf*)(xb + (f - XF0 + 2) * 32);
    if constexpr (MODE != 2 && f >= XF0 && f < XF0 + 8) {
      constexpr int x = f - XF0;
      const v4s lo = __builtin_bit_cast(v4s, __builtin_shufflevector(go[x], go[x], 0, 1, 2, 3)), hi = __builtin_bit_cast(v4s, __builtin_shufflevector(go[x], go[x], 4, 5, 6, 7));
      gr0 = __builtin_amdgcn_mfma_f32_4x4x4bf16_1k(lo, lo, gr0, 0, 0, 0);
      xq = MFMA16(xreg[x], go[x], xq);
      gr0 = __builtin_amdgcn_mfma_f32_4x4x4bf16_1k(hi, hi, gr0, 0, 0, 0);
    }
    if constexpr (f == XF0 + 10 && MODE != 2) {
      float g0 = gr0[0], g1 = gr0[1], g2 = gr0[2], g3 = gr0[3];
      g0 += 0.f; g2 += 0.f;
      const float e0 = LM(0xAAAAAAAAAAAAAAAAull) ? g1 : g0, e1 = LM(0xAAAAAAAAAAAAAAAAull) ? g3 : g2;
      const float s2 = LM(0xCCCCCCCCCCCCCCCCull) ? e1 : e0;
      const float var = vmax(__builtin_fmaf(xq[0] * (-1.f / 128.f), xq[0], add32(s2)), 0.f) * (cc * cc * (1.f / 128.f));
      rho = cc * __builtin_amdgcn_rsqf(var + 1e-5f);
      sh = (LM(0x0101010101010101ull) ? ca * (128.f / 15.f) : xq[0]) * (7.5f / 128.f);
    }
    if constexpr (f == 30 && MODE != 2) {
      *(LAS float*)scw = LM(0xFFFFFFFF00000000ull) ? rho * xq[0] * (-1.f / 128.f) : rho; rl = *(LAS v4f*)scr; ga = *(LAS v4f*)(scr + 128);
      wmean = *(LAS float*)(T_X + 1280);
    }
    if constexpr (MODE != 2) {
      if constexpr (f == 33) ga *= xq[1];
      if constexpr (f == 36 || f == 37) {
#pragma unroll
        for (int r = 2 * (f - 36); r < 2 * (f - 36) + 2; ++r)
          sc[r] = __builtin_fmaf(dppmerge<0xA, 0xC>(dppmerge<0xA, 0x3>(dppmerge<0x5, 0xC>(S[r], S[4 + r]), S[8 + r]), S[12 + r]), rl[r], ga[r]);
      }
      if constexpr (f == 38) {
        *(LAS float*)scw = xq[3]; ga = *(LAS v4f*)scr; gb = *(LAS v4f*)(scr + 128);
        t1 = vmax32(vmax(vmax3(sc[0], sc[1], sc[2]), sc[3])) * rho;
      }
      if constexpr (f == 39 || f == 40) {
#pragma unroll
        for (int r = 2 * (f - 39); r < 2 * (f - 39) + 2; ++r) sc[r] = __builtin_amdgcn_exp2f(__builtin_fmaf(sc[r], rho, -t1));
      }
      if constexpr (f == 43) t3 = add32((sc[0] + sc[1]) + (sc[2] + sc[3]));
      if constexpr (f == 44) { t3 = __builtin_amdgcn_rcpf(t3 * cc); sc *= rl; }
      if constexpr (f == 45) { sc *= t3; p0 = pkbf(sc[0], sc[1]); p1 = pkbf(sc[2], sc[3]); }
      if constexpr (f == 46) t1 = __builtin_fmaf(sc[3], ga[3], __builtin_fmaf(sc[2], ga[2], __builtin_fmaf(sc[1], ga[1], sc[0] * ga[0])));
      if constexpr (f == 47) t3 = __builtin_fmaf(sc[3], gb[3], __builtin_fmaf(sc[2], gb[2], __builtin_fmaf(sc[1], gb[1], __builtin_fmaf(sc[0], gb[0], xq[2]))));
      if constexpr (f == 48) t1 = xq[0] + add32(t1);
      if constexpr (f == 51) t3 = __builtin_fmaf(-t1, wmean, add32(t3));
      if constexpr (f == 52) { t3 *= cc * 1.4426950408889634f; t1 = t1 * t1 * (-1.f / 128.f); asm volatile("" : "+v"(p0), "+v"(p1), "+v"(t1), "+v"(t3)); }
    }
    if constexpr (MODE == 0) {
      if constexpr (f == 59 || f == 60) { mkpa(f - 59); asm volatile("" : "+v"(pa[f - 59])); }
      if constexpr (f == 61 || f == 62) { mkda(f - 61); asm volatile("" : "+v"(da[f - 61])); }
      if constexpr (f == 61) rdgt(0);
    }
    if constexpr ((f & 7) == 7) TP(1 + (f >> 3));
    if constexpr (f >= 32) __builtin_amdgcn_sched_barrier(0); else PIN();
  });
  if constexpr (MODE != 0) {
    const int lx = lane_again(), slot = (lx & 15) | ((lx >> 1) & 16);
    if constexpr (MODE == 1) {
      if (!(lx & 16)) { *(LAS v2u*)(T_SCR + bi * 256 + slot * 8) = (v2u){p0, p1}; *(LAS v2f*)(T_HAND + (lx & 15) * 8) = (v2f){t1, t3}; *(LAS float*)(T_HAND + 136 + (lx & 15) * 4) = sh; }
      CFENCE(); WAITLG0(); CFENCE();
      *(volatile LAS unsigned*)(T_HAND + 128) = 1u;
      return;
    } else {
#pragma unroll 1
      for (unsigned sp = 0; sp < SPINCAP && __builtin_amdgcn_readfirstlane(*(volatile LAS unsigned*)(T_HAND + 128)) == 0; ++sp) __builtin_amdgcn_s_sleep(2);
      CFENCE();
      const v2u pp = *(LAS v2u*)(T_SCR + bi * 256 + slot * 8);
      const v2f tt = *(LAS v2f*)(T_HAND + (lx & 15) * 8);
      p0 = pp[0]; p1 = pp[1]; t1 = tt[0]; t3 = tt[1]; sh = *(LAS float*)(T_HAND + 136 + (lx & 15) * 4);
    }
  }
  if constexpr (MODE == 2) { mkpa(0); mkda(0); rdgt(0); }
  v16f O[4], q;
#define SB() __builtin_amdgcn_sched_barrier(0)
  sfor<0, 4>([&](auto Ti) __attribute__((always_inline)) NOPK {
    constexpr int T = decltype(Ti)::v;
    auto sq = [&](int i0, int i1) __attribute__((always_inline)) NOPK {
      if constexpr (T > 0) {
#pragma unroll
        for (int i = i0; i < i1; ++i) q[i] = T == 1 ? O[0][i] * O[0][i] : __builtin_fmaf(O[T - 1][i], O[T - 1][i], q[i]);
      }
    };
    if constexpr (T < 3) rdgt(T + 1);
    O[T] = MFMA32(pa[0], zb[T][0], zero);
    SB();
    if constexpr (NG == 2) O[T] = MFMA32(pa[1], zb[T][1], O[T]);
    if constexpr (T == 0) { zb[3][0] = pack8(acc[7], 0); asm volatile("" : "+v"(zb[3][0])); }
    sq(0, 5);
    SB();
    O[T] = MFMA32(da[0], gt[T][0], O[T]);
    if constexpr (T == 0 && NG == 2) { zb[3][1] = pack8(acc[7], 8); asm volatile("" : "+v"(zb[3][1])); }
    sq(5, 10);
    SB();
    if constexpr (NG == 2) O[T] = MFMA32(da[1], gt[T][1], O[T]);
    sq(10, 16);
    if constexpr (T > 0) asm volatile("" : "+v"(q));
    SB();
  });
  if constexpr (MODE == 0) __builtin_amdgcn_s_setprio(0);
  TP(9);
  const int l3 = lane_again();
  q += O[3] * O[3];
  float q8[8], q4[4], q2[2];
#pragma unroll
  for (int i = 0; i < 8; ++i) q8[i] = swapadd16(q[i], q[8 + i]);
#define RADD(d, s, ctl, bm) "v_add_f32_dpp " d ", " s ", " s " " ctl " row_mask:0xf bank_mask:" bm "\n\t"
  asm("s_nop 1\n\t" RADD("%0", "%6", "row_ror:8", "0x3") RADD("%1", "%7", "row_ror:8", "0x3") RADD("%2", "%8", "row_ror:8", "0x3") RADD("%3", "%9", "row_ror:8", "0x3")
      RADD("%0", "%10", "row_ror:8", "0xc") RADD("%1", "%11", "row_ror:8", "0xc") RADD("%2", "%12", "row_ror:8", "0xc") RADD("%3", "%13", "row_ror:8", "0xc")
      RADD("%4", "%0", "row_half_mirror", "0x5") RADD("%5", "%1", "row_half_mirror", "0x5") RADD("%4", "%2", "row_half_mirror", "0xa") RADD("%5", "%3", "row_half_mirror", "0xa")
      : "=&v"(q4[0]), "=&v"(q4[1]), "=&v"(q4[2]), "=&v"(q4[3]), "=&v"(q2[0]), "=&v"(q2[1])
      : "v"(q8[0]), "v"(q8[1]), "v"(q8[2]), "v"(q8[3]), "v"(q8[4]), "v"(q8[5]), "v"(q8[6]), "v"(q8[7]));
  float qs = (LM(0xCCCCCCCCCCCCCCCCull) ? q2[1] : q2[0]) + dppf<DPP_X2>(LM(0xCCCCCCCCCCCCCCCCull) ? q2[0] : q2[1]);
  qs += dppf<DPP_X1>(qs);
  qs = bperm(32 * (l3 & 4) + 4 * (l3 & 24) + 8 * (l3 & 3), qs);
  asm volatile("" : "+v"(qs));
  TP(10);
  const float a = t3 * __builtin_amdgcn_rsqf(__builtin_fmaf(vmax(qs + t1, 0.f), cc * (cc * (1.f / 128.f)), 1e-5f));
  float m;
  asm("s_nop 1\n\tv_max_f32_dpp %0, %1, %1 quad_perm:[1,0,3,2] row_mask:0xf bank_mask:0xf\n\ts_nop 1\n\tv_max_f32_dpp %0, %0, %0 quad_perm:[2,3,0,1] row_mask:0xf bank_mask:0xf\n\t"
      "s_nop 1\n\tv_max_f32_dpp %0, %0, %0 row_half_mirror row_mask:0xf bank_mask:0xf" : "=&v"(m) : "v"(a));
  const float e = __builtin_amdgcn_exp2f(a - m);
  const float ec = e * cc;
  float sm = e + dppf<DPP_X1>(e), so = ec * sh;
  so += dppf<DPP_X1>(so); sm += dppf<DPP_X2>(sm); so += dppf<DPP_X2>(so); sm += dppf<DPP_HM>(sm); so += dppf<DPP_HM>(so);
  *(LAS float*)(T_SCR + bi * 256 + l3 * 4) = (LM(0xFFFFFFFF00000000ull) ? so : ec) * __builtin_amdgcn_rcpf(sm);
  LAS char* const gr = ldsbase(T_SCR + bi * 256 + (LM(0xFFFFFFFF00000000ull) ? 16 : 0));
  v4f gam[2 * NG];
  float of[2 * NG];
#pragma unroll
  for (int k = 0; k < 2 * NG; ++k) { gam[k] = *(LAS v4f*)(gr + 32 * k); of[k] = *(LAS float*)(gr + 128 + 32 * k); }
  TP(11);
  float y[NG][4];
#pragma unroll
  for (int T = 0; T < 4; ++T) {
    float v[2 * NG];
#pragma unroll
    for (int k = 0; k < 2 * NG; ++k) v[k] = __builtin_fmaf(gam[k][3], O[T][4 * k + 3], __builtin_fmaf(gam[k][2], O[T][4 * k + 2], __builtin_fmaf(gam[k][1], O[T][4 * k + 1], __builtin_fmaf(gam[k][0], O[T][4 * k], of[k]))));
#pragma unroll
    for (int k = 0; k < NG; ++k) y[k][T] = swapadd32(v[2 * k], v[2 * k + 1]);
  }
  TP(12);
  LAS char* const ow = ldsbase(T_TOK + bi * 32 + (l3 >> 5) * 6400 + ((l3 >> 3) & 3) * 400 + (l3 & 7) * 2);
#pragma unroll
  for (int k = 0; k < NG; ++k) {
    const unsigned p01 = pkbf(y[k][0], y[k][1]), p23 = pkbf(y[k][2], y[k][3]);
    *(LAS u16*)(ow + k * T_HS) = (u16)p01; *(LAS u16*)(ow + k * T_HS + 1600) = (u16)(p01 >> 16);
    *(LAS u16*)(ow + k * T_HS + 3200) = (u16)p23; *(LAS u16*)(ow + k * T_HS + 4800) = (u16)(p23 >> 16);
  }
  const v4u res = *(LAS v4u*)(T_TOK + bi * 32 + (l3 >> 5) * T_HS + (l3 & 31) * 400);
  const unsigned nd = LM(0xFFFFFFFF00000000ull) ? (NG == 2 ? nd23 : ~0u) : nd01;
  store16(res, mkrsrc(out, NN * 256), (LM(0xFFFF0000FFFF0000ull) ? nd >> 16 : nd & 0xFFFFu) * 256, (l3 & 15) * 16, OUTAUX);
  TP(13);
}
__global__ void NOPK __launch_bounds__(1024, 4) k_main(char* ws, const u16* __restrict__ x, const u16* __restrict__ wr, u16* __restrict__ out) {
  const int b = BID, slice = (b & 7) * 4 + (b >> 6), part = (b >> 3) & 7;
  const int tid = TID, wave = __builtin_amdgcn_readfirstlane(tid >> 6), lane = tid & 63, l16 = lane * 16;
  const int swave_ = wave; (void)swave_;
  long long pc0;
  asm volatile("s_getpc_b64 %0" : "=s"(pc0));
  STAMPA(1, 0);
  const __amdgpu_buffer_rsrc_t rs = mkrsrc(ws + WS_B, 8 * HOPB), rh = mkrsrc(ws + WS_LIST, HANDB);
  const int c0 = part * 25 + wave, c1 = c0 + 16;
  const bool has1 = wave < 9;
  if (tid == 0) *(LAS v2u*)H_SYNC = (v2u){(1u << 24) | TCH, 0u};
  const int mvoff = ((lane & 1) && has1 ? 16 * 128 : 0) + (lane & 2) * 2, msoff = H_META + c0 * 128;
  unsigned meta = ~0u;
  int dur = TCL;
  if (!RARE(b >= NCH)) build_lists(ws, out, b, slice, tid, lane, wave, mvoff, msoff, meta, dur);
  else {
    if (b < NCH + 16) touch_code<2>(pc0, tid, 32768);
#pragma unroll
    for (int i = 0; i < 7; ++i)
      if (i < 6 || wave < 4) dma1k(ws + WS_B + slice * SLB + (wave + 16 * i) * 1024 + l16, TB + (wave + 16 * i) * 1024);
      if (wave < 2) warm_up(ws, out, tid, []() {});
    WAITVM0();
    LBAR();
    __builtin_amdgcn_s_sleep(32);
  }
  STAMPA(1, 6);
  float d0 = ((const float*)(ws + WS_PDIS))[c0 * 64 + lane];
  float d1 = ((const float*)(ws + WS_PDIS))[(has1 ? c1 : c0) * 64 + (wave == 8 ? lane & 31 : lane)];
  if (anyl(meta == ~0u)) {
    unsigned sp = 0;
#pragma unroll 1
    do {
      __builtin_amdgcn_s_sleep(4);
      CFENCE();
      meta = __builtin_amdgcn_raw_buffer_load_b32(rh, mvoff, msoff, AUX_SC1);
    } while (anyl(meta == ~0u) & (++sp < SPINCAP));
  }
  const int n0 = __builtin_amdgcn_readlane(meta, 0), n1 = __builtin_amdgcn_readlane(meta, 1);
  const unsigned tl0 = __builtin_amdgcn_readlane(meta, 2) + dur + 36 + TCW, tl1 = __builtin_amdgcn_readlane(meta, 3) + dur + 36 + TCX;
  v8bf sel;
  { const int g = lane >> 4, row = lane & 15;
#pragma unroll
    for (int j = 0; j < 8; ++j) sel[j] = (__bf16)(((row >> 2) == 2 * (g & 1) + (j >> 2) && (j & 1) == (row & 1)) ? 1.0f : 0.0f); }
  const int sidx = (lane & 2) ? 0xEEEE : 0x4444;
  const int tp = b & 7, t0 = (b >> 3) * 782 / 32, nt = ((b >> 3) + 1) * 782 / 32 - t0, pos0 = tp * 1600 + 2 * t0;
  unsigned tspin = 0, nd01, nd23;
  float cc;
  TfAddr ad;
  v8bf mreg[RF];
  auto hops = [&](auto role) __attribute__((always_inline)) NOPK {
    constexpr bool HAS1 = decltype(role)::v;
    int yo[4], xo[2];
    float xs[2];
    const __amdgpu_buffer_rsrc_t rx = mkrsrc(x, NN * 256);
    v4u L1[8];
    int o1a = 0, o1b = 0;
    if constexpr (HAS1) {
#pragma unroll
      for (int t = 0; t < 4; ++t) {
        const int xk = (t & 1) * 576 + tid, per = t < 2 ? 128 : 144, pj = xk / per;
        yo[t] = xk < 7 * per ? (((part + 1 + pj) & 7) * 25 + (t < 2 ? 16 : 20)) * 512 + (xk - pj * per) * 16 : OOB;
      }
    } else {
      yo[0] = ((part + wave - 8) & 7) * 25 * 512 + l16;
#pragma unroll
      for (int t = 0; t < 2; ++t) {
        const int xk = t * 448 + tid - 576, s = xk / 25;
        yo[1 + t] = xk < 800 ? s * SLB + pos0 * 8 + (xk - s * 25) * 16 : OOB;
        xs[t] = ((const float*)(ws + WS_PSH))[pos0 + umin(xk >> 4, 49u)];
        xo[t] = ((const u16*)(ws + WS_INV))[pos0 + umin(xk >> 4, 49u)] * 256 + (lane & 15) * 16;
      }
    }
    STAMPG(1, 13);
    wait_until(tl0);
    STAMPG(1, 14);
    v4u L0[8];
    v2u ov0;
    issue_lists(L0, ov0, rh, c0, n0, l16);
    v2u ov1;
    if constexpr (HAS1) { wait_until(tl1); issue_lists(L1, ov1, rh, c1, n1, l16); }
    check_lists(L0, ov0, ws, c0, n0, l16);
    const int o0a = __builtin_amdgcn_readfirstlane(ov0[0]), o0b = __builtin_amdgcn_readfirstlane(ov0[1]);
    d0 *= d0; d1 *= d1;
    STAMPA(1, 7);
    STAMPS3(unsigned acc_[8] = {}; unsigned hs_ = 0);
    v4u TK[2];
    unsigned wv = 0;
    if constexpr (!HAS1) wv = ((const unsigned*)wr)[lane];
#pragma unroll 1
    for (int k = 1;; ++k) {
      if (HAS1 && k > 1 && tid == 0) __builtin_amdgcn_raw_buffer_store_b32(now(), rh, H_FLAG + (((k - 1) * 8 + part) * 32 + slice) * 128, 0, AUX_SC1);
      const int hb = k * HOPB + slice * SLB;
      v2u r0, r1;
      v4u X[8];
      unsigned fl;
      int fo;
      if constexpr (!HAS1) {
        asm volatile("" : "=v"(TK[0]), "=v"(TK[1]));
        if (k > 3) {
          TK[0] = load16(rs, yo[1], (k - 3) * HOPB, AUX_SC1);
          TK[1] = load16(rs, yo[2], (k - 3) * HOPB, AUX_SC1);
        } else if (k == 3) {
          TK[0] = load16(rx, xo[0], 0, 0);
          TK[1] = load16(rx, xo[1], 0, 0);
        } else if (k == 2) {
          dma1k(ws + WS_WF + (wave - 9) * 1024 + l16, T_PARK + (wave - 9) * 1024);
          if (wave < 9 + RF - 7) dma1k(ws + WS_WF + (wave - 2) * 1024 + l16, T_PARK + (wave - 2) * 1024);
        }
      }
      __builtin_amdgcn_s_setprio(HAS1 ? PRA : PRL);
      {
        v4f a = gather(L0, n0, sel, sidx);
        if (!HAS1) __builtin_amdgcn_s_setprio(2);
        if (RARE(o0a | o0b)) a = slow_edges(a, ws, c0, o0a, o0b, lane);
        WAITVM0();
        if constexpr (!HAS1) {
          fo = lane < 32 || k < 3 ? (((k - 1) * 8 + (lane & 7)) * 32 + slice) * 128 : (((k - 2) * 8 + tp) * 32 + (lane & 31)) * 128;
          asm volatile("s_cmp_lg_u32 %3, 9\n\ts_cbranch_scc1 Lfl%=\n\tglobal_load_dword %0, %1, %2 sc1\nLfl%=:" : "=&v"(fl) : "v"(fo), "s"(ws + WS_LIST + H_FLAG), "s"(k > 1 && k < 7 ? wave : 0) : "memory", "scc");
        }
        r0 = publish(a, d0, rs, hb + c0 * 512, lane);
      }
      if (HAS1 && k < 6) {
        store16(((v4u){~0u, ~0u, ~0u, ~0u}), rs, l16 + (lane < 32 ? 0 : 16 * 512 - 512), hb + 2 * HOPB + c0 * 512, AUX_SC1);
        if (k == 1) store16(((v4u){~0u, ~0u, ~0u, ~0u}), rs, l16 + (lane < 32 ? 0 : 16 * 512 - 512), hb + HOPB + c0 * 512, AUX_SC1);
      }
      STAMPC(8 * k - 6);
      unsigned sy;
      if constexpr (HAS1) {
        if (k == 1) {
          check_lists(L1, ov1, ws, c1, n1, l16);
          o1a = __builtin_amdgcn_readfirstlane(ov1[0]); o1b = __builtin_amdgcn_readfirstlane(ov1[1]);
        }
        __builtin_amdgcn_s_setprio(PRB);
        v4f a = gather(L1, n1, sel, sidx);
        __builtin_amdgcn_s_setprio(2);
        sy = *(volatile LAS unsigned*)H_SYNC;
        if (RARE(o1a | o1b)) a = slow_edges(a, ws, c1, o1a, o1b, lane);
        r1 = publish(a, d1, rs, hb + c1 * 512, lane);
        STAMPC(8 * k - 5);
        if (k == 7) break;
        LBAR();
      } else {
        if (k == 7) break;
        if (k > 1) {
          if (wave == 9) {
            asm volatile("s_waitcnt vmcnt(1)" : "+v"(fl) :: "memory");
            if (RARE(anyl(fl == ~0u))) fl = wait_flags(rh, fo);
            *(LAS unsigned*)H_SYNC = (unsigned)(k << 24) | (wave_max(tdiff(fl, __builtin_amdgcn_readlane(fl, part))) & 0xFFFFFF);
          } else if (wave < 13) __builtin_amdgcn_s_sleep(XSLEEP); else __builtin_amdgcn_s_sleep(XSLEEP2);
        }
        if (k < 6 && (lane < 32 || k == 1)) store16(((v4u){~0u, ~0u, ~0u, ~0u}), rs, lane < 32 ? l16 + HOPB : l16 - 512, hb + HOPB + c0 * 512, AUX_SC1);
        auto lagword = [&]() __attribute__((always_inline)) NOPK {
#pragma unroll 1
          for (unsigned sp = 0; sp < SPINCAP; ++sp) {
            sy = __builtin_amdgcn_readfirstlane(*(volatile LAS unsigned*)H_SYNC);
            if ((int)(sy >> 24) == k) break;
            __builtin_amdgcn_s_sleep(2);
          }
        };
        if (k == 1) { LBAR(); nap(TCG); }
        if (k == 2) lagword();
        CFENCE();
#pragma unroll
        for (int i = 0; i < NA1; ++i) X[i] = load16(rs, yo[0], hb + i * 1024, AUX_SC1);
        STAMPC(8 * k - 5);
        bool late = true;
        if (k > 1) {
          lagword();
          late = (sy & 0xFFFFFF) > TC0;
          if (late) { LBAR(); nap((sy & 0xFFFFFF) - TC0); }
        }
        STAMPC(8 * k + 1);
#pragma unroll
        for (int i = 4; i < 6; ++i) X[i] = load16(rs, yo[0], hb + i * 1024, AUX_SC1);
        if (!late) LBAR();
        STAMPC(8 * k);
#pragma unroll
        for (int i = 6; i < 8; ++i) X[i] = load16(rs, yo[0], hb + i * 1024, AUX_SC1);
      }
      STAMPC(8 * k - 4);
      if (k == 1) STAMPA(1, 18);
      constexpr int NP = HAS1 ? 4 : 8;
      unsigned bad = 0;
      if constexpr (HAS1) {
        sy = __builtin_amdgcn_readfirstlane(sy);
        if ((int)(sy >> 24) != k) sy = __builtin_amdgcn_readfirstlane(*(volatile LAS unsigned*)H_SYNC);
        sy &= 0xFFFFFF;
        if (sy > TC1) { nap(sy - TC1); sy = TC1; }
        STAMPC(8 * k);
        asm volatile("" : "=v"(X[1]), "=v"(X[3]));
        X[0] = load16(rs, yo[0], hb, AUX_SC1);
        if (wave < 5) X[1] = load16(rs, yo[1], hb, AUX_SC1);
        CFENCE();
        *(LAS v2u*)(TB + c0 * 512 + lane * 8) = r0;
        *(LAS v2u*)(TB + c1 * 512 + lane * 8) = r1;
        nap(sy + TC2);
        X[2] = load16(rs, yo[2], hb, AUX_SC1);
        if (wave < 7) X[3] = load16(rs, yo[3], hb, AUX_SC1);
        STAMPC(8 * k - 1);
#pragma unroll
        for (int t = 0; t < 4; ++t) {
          if (!(t & 1) || yo[t] != OOB) {
            if (max4(X[t]) == ~0u) bad |= 1u << t;
            *(LAS v4u*)(TB + yo[t]) = X[t];
          }
          CFENCE();
        }
      } else {
        LAS char* const xl = ldsbase(TB + yo[0]);
        CFENCE();
        *(LAS v2u*)(TB + c0 * 512 + lane * 8) = r0;
        CFENCE();
#pragma unroll
        for (int i = 0; i < 8; ++i) {
          if (max4(X[i]) == ~0u) bad |= 1u << i;
          *(LAS v4u*)(xl + i * 1024) = X[i];
          CFENCE();
        }
        if (k > 3) {
          *(LAS v4u*)(T_TOK + (k - 3) * T_HS + (tid - 576) * 16) = TK[0];
          if (yo[2] != OOB) *(LAS v4u*)(T_TOK + (k - 3) * T_HS + (tid - 576 + 448) * 16) = TK[1];
        } else if (k == 3) {
          const int o = T_TOK + (lane & 15) * 800 + ((tid - 576) >> 4) * 8;
          if (RARE(anyl(xs[0] != 0.f || xs[1] != 0.f))) {
#pragma unroll
            for (int i = 0; i < 8; ++i) { const unsigned u = TK[i >> 2][i & 3]; TK[i >> 2][i & 3] = pkbf(bflo(u) - xs[i >> 2], bfhi(u) - xs[i >> 2]); }
          }
          *(LAS v2u*)(o) = (v2u){TK[0][0], TK[0][1]}; *(LAS v2u*)(o + 400) = (v2u){TK[0][2], TK[0][3]};
          if (yo[2] != OOB) { *(LAS v2u*)(o + 224) = (v2u){TK[1][0], TK[1][1]}; *(LAS v2u*)(o + 624) = (v2u){TK[1][2], TK[1][3]}; }
        }
        STAMPC(8 * k - 1);
      }
      if (RARE(anyl(bad != 0))) {
        unsigned spins = 0;
#pragma unroll 1
        do {
          CFENCE();
#pragma unroll
          for (int i = 0; i < NP; ++i) X[i] = load16(rs, (bad >> i) & 1 ? yo[HAS1 ? i & 3 : 0] : OOB, hb + (HAS1 ? 0 : i * 1024), AUX_SC1);
          ++spins;
#pragma unroll
          for (int i = 0; i < NP; ++i)
            if (((bad >> i) & 1) && (max4(X[i]) != ~0u || spins >= SPINCAP)) { *(LAS v4u*)(ldsbase(TB + yo[HAS1 ? i & 3 : 0]) + (HAS1 ? 0 : i * 1024)) = X[i]; bad ^= 1u << i; }
          if (anyl(bad != 0)) __builtin_amdgcn_s_sleep(4);
        } while (anyl(bad != 0));
        tspin += spins;
      }
      STAMPC(8 * k - 3);
      WAITVM0();
      LBAR();
      STAMPC(8 * k - 2);
      STAMPS3(hs_ = (unsigned)__builtin_amdgcn_s_memtime());
    }
    STAMPS3(for (int i = 0; i < 8; ++i) STAMPV(4, i, acc_[i] / 4));
    if constexpr (HAS1) asm volatile("ds_max_u32 %0, %1" :: "v"(lane == 0 ? H_SYNC + 4 : LDS_NONE), "v"(now()) : "memory");
    const int p = pos0 + (wave < 12 ? 4 * wave : 48);
    const float ca = ((const float*)(ws + WS_PCI))[p + ((lane >> 3) & (wave < 12 ? 3 : 1)) + ((lane & 7) ? 0 : (WS_PSH - WS_PCI) / 4)];
    const unsigned ndv = ((const unsigned*)(ws + WS_INV + p * 2))[lane & 1];
    LAS char* const pkb = ldsbase(T_PARK + l16);
#pragma unroll
    for (int f = 0; f < RF; ++f) mreg[f] = *(LAS v8bf*)(pkb + f * 1024);
    if constexpr (HAS1) {
      STAMPA(3, 3);
      LBAR();
      STAMPA(3, 5);
      const char* src = ws + WS_WF + RF * 1024 + l16;
      if (wave >= 4) {
#pragma unroll 1
        for (int i = wave - 4; i < 32 - RF; i += 5) dma1k(src + i * 1024, T_W + i * 1024);
      }
      ad = tf_addr(wave, false, lane);
      WAITVM0();
      if (wave < 4) {
        nap(TCW2);
#pragma unroll 1
        for (int i = 32 - RF + wave; i < 64 - RF; i += 4) dma1k(src + i * 1024, T_W + i * 1024);
      }
    } else {
      const unsigned f = wait_flags(rh, lane < 32 ? ((6 * 8 + tp) * 32 + lane) * 128 : ((6 * 8 + part) * 32 + slice) * 128);
      const int lag = wave_max(tdiff(f, __builtin_amdgcn_readlane(f, 32)));
      v4u X[4];
#pragma unroll
      for (int i = 0; i < 4; ++i) X[i] = load16(rs, yo[1 + (i & 1)], (5 + (i >> 1)) * HOPB, AUX_SC1);
      const v2u mx = *(LAS v2u*)(H_VEC + (tid & 127) * 8);
      ad = tf_addr(wave < 12 ? wave : 12, wave >= 12, lane);
      float sw = bflo(wv) + bfhi(wv);
      sw += dppf<DPP_X1>(sw); sw += dppf<DPP_X2>(sw); sw += dppf<DPP_HM>(sw); sw += dppf<DPP_RM>(sw);
      sw = add32(add16(sw)) * (1.f / 128.f);
      STAMPA(3, 4);
      LBAR();
      const unsigned tend7 = __builtin_amdgcn_readfirstlane(*(LAS unsigned*)(H_SYNC + 4)) + lag;
      LAS char* const tw = ldsbase(T_TOK + 4 * T_HS + (tid - 576) * 16);
#pragma unroll
      for (int i = 0; i < 6; ++i)
        if (!(i & 1) || yo[2] != OOB) *(LAS v4u*)(tw + (3 - (i >> 1)) * T_HS + (i & 1) * 448 * 16) = i < 2 ? TK[i] : X[i - 2];
      if (wave >= 14) {
        const int q = tid - 896;
        if (q < 96) *(LAS v2u*)(T_X + ((q >> 5) ? (q >> 5) + 2 : 1) * 256 + (q & 31) * 8) = mx;
        *(LAS unsigned*)(T_X + (wave == 14 ? 0 : 512) + lane * 4) = wave == 14 ? 0x3F803F80u : wv;
        if (tid == 1023) { *(LAS float*)(T_X + 1280) = sw; *(LAS v2u*)(T_HAND + 128) = (v2u){0u, 0u}; }
      }
      wait_until(tend7 + TC7);
      STAMPA(3, 6);
      X[0] = load16(rs, yo[1], 7 * HOPB, AUX_SC1);
      X[1] = load16(rs, yo[2], 7 * HOPB, AUX_SC1);
      if (RARE(anyl(umax(max4(X[0]), max4(X[1])) == ~0u))) {
#pragma unroll 1
        for (unsigned sp = 0; sp < SPINCAP; ++sp) {
          const bool b0 = max4(X[0]) == ~0u, b1 = max4(X[1]) == ~0u;
          if (!anyl(b0 || b1)) break;
          __builtin_amdgcn_s_sleep(4);
          CFENCE();
          if (b0) X[0] = load16(rs, yo[1], 7 * HOPB, AUX_SC1);
          if (b1) X[1] = load16(rs, yo[2], 7 * HOPB, AUX_SC1);
        }
      }
      STAMPA(3, 7);
      *(LAS v4u*)(tw) = X[0];
      if (yo[2] != OOB) *(LAS v4u*)(tw + 448 * 16) = X[1];
    }
    cc = ca;
    nd01 = __builtin_amdgcn_readlane(ndv, 0); nd23 = __builtin_amdgcn_readlane(ndv, 1);
  };
  if (has1) hops(IC<1>{}); else hops(IC<0>{});
  STAMPA(3, 0);
  STAMPV(3, 58, tspin);
  LBAR();
  STAMPA(3, 1); STAMPV(3, 60, __builtin_amdgcn_s_memtime());
  int wv2 = wave;
  auto forget = [&]() __attribute__((always_inline)) NOPK {
#pragma unroll
    for (int f = 0; f < RF; ++f) asm volatile("" : "=v"(mreg[f]));
    asm volatile("" : "=v"(cc), "=v"(ad.go), "=v"(ad.xb), "=v"(ad.wb), "=v"(ad.sc), "=v"(ad.key), "=v"(ad.tr));
  };
  if (wv2 < 12) { tf_batch<0>(wv2, ad, mreg, cc, nd01, nd23, ws, out); forget(); }
  asm volatile("" : "+s"(wv2));
  if (wv2 == 13 && nt == 25) { tf_batch<1>(12, ad, mreg, cc, nd01, nd23, ws, out); forget(); }
  asm volatile("" : "+s"(wv2));
  if (wv2 == 14 && nt == 25) tf_batch<2>(12, ad, mreg, cc, nd01, nd23, ws, out);
  STAMPA(3, 2); STAMPV(3, 61, __builtin_amdgcn_s_memtime());
}

extern "C" void kernel_launch(void* const* d_in, const int* in_sizes, int n_in, void* d_out, int out_size, void* d_ws, size_t ws_size, hipStream_t stream) {
  (void)in_sizes; (void)n_in; (void)out_size; (void)ws_size;
  char* ws = (char*)d_ws;
  (void)hipFuncSetAttribute((const void*)k_prep, hipFuncAttributeMaxDynamicSharedMemorySize, K1_LDS);
  (void)hipFuncSetAttribute((const void*)k_main, hipFuncAttributeMaxDynamicSharedMemorySize, KM_LDS);
  k_prep<<<G1 + 32, 1024, K1_LDS, stream>>>((const u16*)d_in[0], (const int*)d_in[1], (const int*)d_in[2], (const u16*)d_in[3],
                                           (const u16*)d_in[4], (const u16*)d_in[5], (const u16*)d_in[6], (const u16*)d_in[7], (const u16*)d_in[8], ws, (const u16*)d_out);
  k_main<<<256, 1024, KM_LDS, stream>>>(ws, (const u16*)d_in[0], (const u16*)d_in[8], (u16*)d_out);
}
```
